```python
import torch

def _softmax(x, axis=-1):
    return torch.softmax(x, dim=axis)
def _gelu(x):
    return torch.nn.functional.gelu(x, approximate="tanh")
def _silu(x):
    return torch.nn.functional.silu(x)
def _sigmoid(x):
    return torch.sigmoid(x)
def _layernorm(x, axis=-1, eps=1e-5):
    m = x.mean(dim=axis, keepdim=True)
    v = x.var(dim=axis, keepdim=True, unbiased=False)
    return (x - m) / torch.sqrt(v + eps)
def _scatter_add(tgt, idx, src, axis=0):
    return tgt.clone().index_add_(axis, idx, src)
def _segment_sum(src, seg, n):
    out = torch.zeros((n,) + src.shape[1:], dtype=src.dtype)
    return out.index_add_(0, seg, src)
def _pad(x, pw):
    flat = tuple(v for pair in reversed(pw) for v in pair)
    return torch.nn.functional.pad(x, flat)

kernel_name = 'parallel_sum__sliding_local_dilated__glu_gated_geglu__prompt'

def setup_inputs():

    _w = (lambda a: (lambda t: t.to(torch.bfloat16) if t.is_floating_point() else t)(torch.as_tensor(a)))
    def _A_setup():
        B, S, D = 1, 2048, 640
        return {
            "x": _w((lambda *s: torch.randn(*s, dtype=torch.bfloat16) * 0.05)(B, S, D)),
            "wqkv": _w((lambda *s: torch.randn(*s, dtype=torch.bfloat16) * 0.05)(D, 3 * D)),
            "wo": _w((lambda *s: torch.randn(*s, dtype=torch.bfloat16) * 0.05)(D, D)),
        }
    _A_in = _A_setup()

    def _B_setup():
        B, S, Dm, Df = 1, 2048, 640, 1728
        return {
            "x": _w((lambda *s: torch.randn(*s, dtype=torch.bfloat16) * 0.05)(B, S, Dm)),
            "w_gate": _w((lambda *s: torch.randn(*s, dtype=torch.bfloat16) * 0.8)(Dm, Df) * (Dm ** -0.5)),
            "w_up": _w((lambda *s: torch.randn(*s, dtype=torch.bfloat16) * 0.8)(Dm, Df) * (Dm ** -0.5)),
            "w_down": _w((lambda *s: torch.randn(*s, dtype=torch.bfloat16) * 0.8)(Df, Dm) * (Df ** -0.5)),
        }
    _B_in = _B_setup()

    return {
        "A_x": _A_in["x"],
        "A_wqkv": _A_in["wqkv"],
        "A_wo": _A_in["wo"],
        "B_x": _B_in["x"],
        "B_w_gate": _B_in["w_gate"],
        "B_w_up": _B_in["w_up"],
        "B_w_down": _B_in["w_down"],
    }

def reference(A_x, A_wqkv, A_wo, B_x, B_w_gate, B_w_up, B_w_down):
    def _A_ref(x, wqkv, wo):
        B, S, D = x.shape
        H = 32; Dh = D // H
        W, dil = 8, 2
        qkv = (lambda x, *d: x.permute(*d))((x @ wqkv).reshape(B, S, 3, H, Dh), 2, 0, 3, 1, 4)
        q, k, v = qkv[0], qkv[1], qkv[2]
        scale = Dh ** -0.5
        pos = torch.arange(S)
        off = torch.arange(W) * dil
        idx = pos[:, None] - off[None, :]
        mask = idx >= 0
        idx_c = torch.where(mask, idx, 0).reshape(-1)
        kg = k[:, :, idx_c, :].reshape(B, H, S, W, Dh)
        vg = v[:, :, idx_c, :].reshape(B, H, S, W, Dh)
        logits = torch.einsum("bhsd,bhswd->bhsw", q, kg) * scale
        logits = torch.where(mask[None, None, :, :], logits, -1e9)
        attn = _softmax(logits, axis=-1)
        o = torch.einsum("bhsw,bhswd->bhsd", attn, vg)
        return (lambda x, *d: x.permute(*d))(o, 0, 2, 1, 3).reshape(B, S, D) @ wo

    def _B_ref(x, w_gate, w_up, w_down):
        g = torch.einsum("bsd,df->bsf", x, w_gate)
        u = torch.einsum("bsd,df->bsf", x, w_up)
        h = _gelu(g) * u
        return torch.einsum("bsf,fd->bsd", h, w_down)

    x = A_x
    xn = _layernorm(x, axis=-1)
    return x + _A_ref(xn, A_wqkv, A_wo) + _B_ref(xn, B_w_gate, B_w_up, B_w_down)
```

```cpp
#include <hip/hip_runtime.h>
#include <hip/hip_bf16.h>
typedef unsigned short u16;
__device__ __forceinline__ float bf2f(u16 v){ return __uint_as_float(((unsigned)v)<<16); }
__device__ __forceinline__ u16 f2bf(float f){ __hip_bfloat16 b = __float2bfloat16(f); return *(u16*)&b; }
__device__ __forceinline__ float rbf(float f){ return bf2f(f2bf(f)); }

constexpr int S=2048, D=640, F=1728, H=32, DH=20;

__global__ void ln_k(const u16* x, u16* xn){
  int row=blockIdx.x; int t=threadIdx.x;
  float v[10]; float s=0;
  for(int i=0;i<10;i++){ v[i]=bf2f(x[row*D+t+64*i]); s+=v[i]; }
  for(int o=32;o;o>>=1) s+=__shfl_xor(s,o);
  float m=s/D; float q=0;
  for(int i=0;i<10;i++){ float d=v[i]-m; q+=d*d; }
  for(int o=32;o;o>>=1) q+=__shfl_xor(q,o);
  float var=rbf(q/D); m=rbf(m);
  float den=rbf(sqrtf(rbf(var+1e-5f)));
  for(int i=0;i<10;i++) xn[row*D+t+64*i]=f2bf(rbf(v[i]-m)/den);
}
__global__ void gemm_k(const u16* A, int lda, const u16* B, int ldb, u16* C, int ldc, int M, int N, int K){
  __shared__ float As[16][65]; __shared__ float Bs[16][65];
  int bm=blockIdx.y*64, bn=blockIdx.x*64; int tx=threadIdx.x&15, ty=threadIdx.x>>4;
  float acc[4][4]={};
  for(int k0=0;k0<K;k0+=16){
    for(int i=threadIdx.x;i<1024;i+=256){ int r=i>>4,c=i&15; As[c][r]=bf2f(A[(bm+r)*lda+k0+c]); }
    for(int i=threadIdx.x;i<1024;i+=256){ int r=i>>6,c=i&63; Bs[r][c]=(bn+c<N)?bf2f(B[(k0+r)*ldb+bn+c]):0.f; }
    __syncthreads();
    for(int k=0;k<16;k++){
      float a[4],b[4];
      for(int i=0;i<4;i++){a[i]=As[k][ty*4+i]; b[i]=Bs[k][tx*4+i];}
      for(int i=0;i<4;i++)for(int j=0;j<4;j++)acc[i][j]+=a[i]*b[j];
    }
    __syncthreads();
  }
  for(int i=0;i<4;i++)for(int j=0;j<4;j++){ int c=bn+tx*4+j; if(c<N) C[(bm+ty*4+i)*ldc+c]=f2bf(acc[i][j]); }
}
__global__ void attn_k(const u16* qkv, u16* o){
  int idx=blockIdx.x*blockDim.x+threadIdx.x; int s=idx/H, h=idx%H;
  float q[DH]; for(int d=0;d<DH;d++) q[d]=bf2f(qkv[s*1920+h*DH+d]);
  float lg[8]; float mx=-1e30f;
  float scale=rbf(0.22360679775f);
  for(int w=0;w<8;w++){ int p=s-2*w; float l;
    if(p>=0){ float a=0; for(int d=0;d<DH;d++) a+=q[d]*bf2f(qkv[p*1920+640+h*DH+d]); l=rbf(rbf(a)*0.22360679775f);} else l=-1e9f;
    lg[w]=l; mx=fmaxf(mx,l);}
  float sum=0; for(int w=0;w<8;w++){ lg[w]=expf(lg[w]-mx); sum+=lg[w]; }
  for(int d=0;d<DH;d++){ float a=0; for(int w=0;w<8;w++){ int p=s-2*w; if(p<0)p=0; a+=rbf(lg[w]/sum)*bf2f(qkv[p*1920+1280+h*DH+d]); } o[s*D+h*DH+d]=f2bf(a);}
}
__global__ void geglu_k(const u16* g, const u16* u, u16* h, int n){
  int i=blockIdx.x*blockDim.x+threadIdx.x; if(i>=n)return;
  float x=bf2f(g[i]); float t=tanhf(0.7978845608f*(x+0.044715f*x*x*x));
  h[i]=f2bf(rbf(0.5f*x*(1.f+t))*bf2f(u[i]));
}
__global__ void final_k(const u16* x,const u16* a,const u16* b,u16* out,int n){
  int i=blockIdx.x*blockDim.x+threadIdx.x; if(i>=n)return;
  out[i]=f2bf(rbf(bf2f(x[i])+bf2f(a[i]))+bf2f(b[i]));
}
extern "C" void kernel_launch(void* const* d_in, const int* in_sizes, int n_in, void* d_out, int out_size, void* d_ws, size_t ws_size, hipStream_t stream) {
  const u16* x=(const u16*)d_in[0]; const u16* wqkv=(const u16*)d_in[1]; const u16* wo=(const u16*)d_in[2];
  const u16* wg=(const u16*)d_in[4]; const u16* wu=(const u16*)d_in[5]; const u16* wd=(const u16*)d_in[6];
  u16* ws=(u16*)d_ws; u16* xn=ws; u16* qkv=xn+S*D; u16* o=qkv+S*1920; u16* g=o+S*D; u16* u=g+S*F; u16* h=u+S*F; u16* a=h+S*F; u16* b=a+S*D;
  ln_k<<<S,64,0,stream>>>(x,xn);
  gemm_k<<<dim3(30,32),256,0,stream>>>(xn,D,wqkv,1920,qkv,1920,S,1920,D);
  attn_k<<<S*H/256,256,0,stream>>>(qkv,o);
  gemm_k<<<dim3(10,32),256,0,stream>>>(o,D,wo,D,a,D,S,D,D);
  gemm_k<<<dim3(27,32),256,0,stream>>>(xn,D,wg,F,g,F,S,F,D);
  gemm_k<<<dim3(27,32),256,0,stream>>>(xn,D,wu,F,u,F,S,F,D);
  geglu_k<<<(S*F+255)/256,256,0,stream>>>(g,u,h,S*F);
  gemm_k<<<dim3(10,32),256,0,stream>>>(h,F,wd,D,b,D,S,D,F);
  final_k<<<S*D/256,256,0,stream>>>(x,a,b,(u16*)d_out,S*D);
}
```

```cpp
#include <hip/hip_runtime.h>
#include <stdint.h>

typedef unsigned short u16;
typedef unsigned int u32;
typedef __attribute__((ext_vector_type(8))) __bf16 bf16x8;
typedef __attribute__((ext_vector_type(4))) __bf16 bf16x4;
typedef __attribute__((ext_vector_type(2))) __bf16 bf16x2;
typedef __attribute__((ext_vector_type(4))) short s16x4;
typedef __attribute__((ext_vector_type(8))) short s16x8;
typedef __attribute__((ext_vector_type(2))) float f32x2;
typedef __attribute__((ext_vector_type(4))) float f32x4;
typedef __attribute__((ext_vector_type(16))) float f32x16;
typedef __attribute__((ext_vector_type(4))) u32 u32x4;
typedef __attribute__((ext_vector_type(2))) u32 u32x2;
#define AS3 __attribute__((address_space(3)))
#define AS1 __attribute__((address_space(1)))
#define DEVI __device__ __forceinline__

constexpr int S_ = 2048, D_ = 640, F_ = 1728;
constexpr int KB2 = 74;
constexpr size_t A2F_BYTES = (size_t)32 * KB2 * 4096;
constexpr int MT_MLP = 121;
constexpr int UT_ATT = 86;

template <int N> DEVI void wait_vm() { asm volatile("s_waitcnt vmcnt(%0)" ::"n"(N) : "memory"); }
template <int N> DEVI void wait_lgkm() { asm volatile("s_waitcnt lgkmcnt(%0)" ::"n"(N) : "memory"); }
DEVI void bar() { __builtin_amdgcn_s_barrier(); }
DEVI void sched() { __builtin_amdgcn_sched_barrier(0); }
DEVI u32 opq(u32 v) { asm volatile("" : "+v"(v)); return v; }

#define STAMP(K, i)
#define STP_DECL
#define STP_ARG
#define STP_PASS
DEVI u32 pk2(float a, float b) {
  f32x2 v = {a, b};
  bf16x2 r = __builtin_convertvector(v, bf16x2);
  return __builtin_bit_cast(u32, r);
}
DEVI float rbf(float a) { return __uint_as_float(pk2(a, 0.f) << 16); }
DEVI float lo16(u32 v) { return __uint_as_float(v << 16); }
DEVI float hi16(u32 v) { return __uint_as_float(v & 0xffff0000u); }

typedef __amdgpu_buffer_rsrc_t rsrc_t;
DEVI rsrc_t mkrs(const char* p, int bytes) { return __builtin_amdgcn_make_buffer_rsrc((void*)p, (short)0, bytes, 0x00020000); }
template <int SOFF, int IMM> DEVI void dma16(rsrc_t rs, u32 voff, u32 lds) {
  u32 tmp;
  asm volatile("s_mov_b32 m0, %1\n\ts_mov_b32 %0, %4\n\tbuffer_load_dwordx4 %2, %3, %0 offen offset:%5 lds"
               : "=&s"(tmp) : "s"(lds), "v"(voff), "s"(rs), "i"(SOFF), "i"(IMM) : "memory");
}
template <int SOFF, int IMM> DEVI void dma16m(rsrc_t rs, u32 voff, u32 lds, uint64_t mask) {
  u32 tmp;
  asm volatile("s_mov_b32 m0, %1\n\ts_mov_b32 %0, %4\n\ts_mov_b64 exec, %6\n\tbuffer_load_dwordx4 %2, %3, %0 offen offset:%5 lds\n\ts_mov_b64 exec, -1"
               : "=&s"(tmp) : "s"(lds), "v"(voff), "s"(rs), "i"(SOFF), "i"(IMM), "s"(mask) : "memory");
}
template <int IMM> DEVI void dma16s(rsrc_t rs, u32 voff, u32 soff, u32 lds) {
  asm volatile("s_mov_b32 m0, %0\n\ts_nop 4\n\tbuffer_load_dwordx4 %1, %2, %3 offen offset:%4 lds"
               :: "s"(lds), "v"(voff), "s"(rs), "s"(soff), "i"(IMM) : "memory");
}
DEVI bf16x8 ld128(AS3 char* p) { return *(AS3 bf16x8*)p; }
DEVI s16x4 ldtr(AS3 char* p) { return __builtin_amdgcn_ds_read_tr16_b64_v4i16((AS3 s16x4*)p); }
DEVI bf16x8 cat(s16x4 a, s16x4 b) {
  s16x8 r = __builtin_shufflevector(a, b, 0, 1, 2, 3, 4, 5, 6, 7);
  return __builtin_bit_cast(bf16x8, r);
}
template <int CTRL> DEVI float dppf(float v) {
  return __int_as_float(__builtin_amdgcn_update_dpp(0, __float_as_int(v), CTRL, 0xf, 0xf, true));
}
DEVI float sum8(float v) {
  v += dppf<0xB1>(v);
  v += dppf<0x4E>(v);
  v += dppf<0x141>(v);
  return v;
}
DEVI float xg_max(float x) {
  auto s = __builtin_amdgcn_permlane16_swap(__float_as_uint(x), __float_as_uint(x), false, false);
  x = fmaxf(__uint_as_float(s[0]), __uint_as_float(s[1]));
  auto t = __builtin_amdgcn_permlane32_swap(__float_as_uint(x), __float_as_uint(x), false, false);
  return fmaxf(__uint_as_float(t[0]), __uint_as_float(t[1]));
}
DEVI float xg_sum(float x) {
  auto s = __builtin_amdgcn_permlane16_swap(__float_as_uint(x), __float_as_uint(x), false, false);
  x = __uint_as_float(s[0]) + __uint_as_float(s[1]);
  auto t = __builtin_amdgcn_permlane32_swap(__float_as_uint(x), __float_as_uint(x), false, false);
  return __uint_as_float(t[0]) + __uint_as_float(t[1]);
}

struct RowStat { float c, s1, s2; };
template <bool FIRST, int I> DEVI void stat_acc(RowStat& st, bf16x8 v8) {
  u32x4 v = __builtin_bit_cast(u32x4, v8);
  if (FIRST && I == 0) { st.c = lo16(v[0]); st.s1 = 0.f; st.s2 = 0.f; }
  float a = lo16(v[I]) - st.c, b = hi16(v[I]) - st.c;
  st.s1 += a; st.s2 = fmaf(a, a, st.s2);
  st.s1 += b; st.s2 = fmaf(b, b, st.s2);
  asm volatile("" : "+v"(st.s1), "+v"(st.s2));
}
DEVI f32x2 stat_fin(const RowStat& st) {
  float d = st.s1 * (1.f / 80.f);
  float mi = st.c + d;
  float m2 = fmaf(-st.s1, d, st.s2);
  float mean = sum8(mi) * 0.125f;
  float e = mi - mean;
  m2 = fmaf(80.f * e, e, m2);
  float var = fmaxf(sum8(m2) * (1.f / 640.f), 0.f);
  float den = rbf(sqrtf(rbf(rbf(var) + 1e-5f)));
  f32x2 r = {rbf(mean), 1.f / den};
  return r;
}

constexpr int M_GR = 0, M_UR = 49152, M_XR = 98304, M_ST = 147456, M_CS = M_ST + 1024;
constexpr int LDS_K1 = 150528;

constexpr int mlp_nd(int s, int a) {
  if (a == 1) return (s + 4 < 20) ? 2 : 0;
  int n = 0;
  if (s >= 1 && s + 3 < 20) n += 1;
  if (s >= 2 && (s - 2) / 2 + 3 < 10) n += 1;
  return n;
}
constexpr int mlp_vm(int s) {
  int n = mlp_nd(s, 0);
  if (s >= 1) n += mlp_nd(s - 1, 0) + mlp_nd(s - 1, 1);
  if (s >= 2) n += mlp_nd(s - 2, 1);
  if (s == 0) n += 2 + 3 + 3 + 2;
  if (s == 1) n += 3 + 2;
  if (s == 2) n += 2;
  return n;
}

struct MlpState {
  f32x16 acc[2][3];
  bf16x8 wf[2][3], xf[2][2];
  RowStat st[2];
  u32 wv[3], xv[2];
  u32 wa[3], xa[4], sa;
  rsrc_t wrs, xrs;
  u32 wdst, xdst;
  uint64_t xmask;
};

template <int SST, int A, int B> DEVI void mlp_rdw(MlpState& t, AS3 char* sm) {
  constexpr int wo = (SST & 3) * 12288 + A * 6144;
  t.wf[A][B] = cat(ldtr(sm + t.wa[B] + wo), ldtr(sm + t.wa[B] + wo + 1536));
}
template <int SST, int A, int I> DEVI void mlp_rdx(MlpState& t, AS3 char* sm) {
  t.xf[A][I] = ld128(sm + t.xa[(SST & 1) * 2 + A] + ((SST >> 1) % 3) * 16384 + I * 4096);
}
template <int WS, int I> DEVI void mlp_dma_w(MlpState& t) {
  dma16<WS * 32 * 3456, I * 1024>(t.wrs, t.wv[I], t.wdst + (WS & 3) * 12288);
}
template <int XS, int I> DEVI void mlp_dma_x(MlpState& t) {
  if constexpr (I == 0) dma16<XS * 128, 0>(t.xrs, t.xv[0], t.xdst + (XS % 3) * 16384);
  else dma16m<XS * 128, 1024>(t.xrs, t.xv[1], t.xdst + (XS % 3) * 16384, t.xmask);
}
template <int A, int I, int B> DEVI void mlp_mm(MlpState& t) {
  sched();
  t.acc[I][B] = __builtin_amdgcn_mfma_f32_32x32x16_bf16(t.wf[A][B], t.xf[A][I], t.acc[I][B], 0, 0, 0);
  sched();
}

template <int SST> DEVI void mlp_stage(MlpState& t, AS3 char* sm) {
  mlp_mm<0, 0, 0>(t);
  mlp_rdx<SST, 1, 0>(t, sm); mlp_rdw<SST, 1, 0>(t, sm);
  mlp_mm<0, 0, 1>(t);
  mlp_rdw<SST, 1, 1>(t, sm); mlp_rdw<SST, 1, 2>(t, sm);
  mlp_mm<0, 0, 2>(t);
  mlp_rdx<SST, 1, 1>(t, sm);
  bf16x8 sv = ld128(sm + t.sa + ((SST >> 1) % 3) * 16384 + (SST & 1) * 1024);
  mlp_mm<0, 1, 0>(t);
  if constexpr (SST >= 1 && SST + 3 < 20) mlp_dma_w<SST + 3, 2>(t);
  mlp_mm<0, 1, 1>(t);
  if constexpr (SST >= 2 && (SST - 2) / 2 + 3 < 10) mlp_dma_x<(SST - 2) / 2 + 3, SST & 1>(t);
  mlp_mm<0, 1, 2>(t);
  if constexpr (SST < 19) {
    wait_lgkm<0>();
    wait_vm<mlp_vm(SST)>();
    bar();
  }
  mlp_mm<1, 0, 0>(t);
  if constexpr (SST < 19) { mlp_rdx<SST + 1, 0, 0>(t, sm); mlp_rdw<SST + 1, 0, 0>(t, sm); }
  mlp_mm<1, 0, 1>(t);
  if constexpr (SST < 19) { mlp_rdw<SST + 1, 0, 1>(t, sm); mlp_rdw<SST + 1, 0, 2>(t, sm); }
  stat_acc<(SST < 2), 0>(t.st[SST & 1], sv);
  mlp_mm<1, 0, 2>(t);
  if constexpr (SST < 19) mlp_rdx<SST + 1, 0, 1>(t, sm);
  stat_acc<(SST < 2), 1>(t.st[SST & 1], sv);
  mlp_mm<1, 1, 0>(t);
  if constexpr (SST + 4 < 20) mlp_dma_w<SST + 4, 0>(t);
  stat_acc<(SST < 2), 2>(t.st[SST & 1], sv);
  mlp_mm<1, 1, 1>(t);
  if constexpr (SST + 4 < 20) mlp_dma_w<SST + 4, 1>(t);
  stat_acc<(SST < 2), 3>(t.st[SST & 1], sv);
  mlp_mm<1, 1, 2>(t);
  if constexpr (SST < 19) mlp_stage<SST + 1>(t, sm);
}

DEVI void k1_mlp(int mt, int fs, const char* x, const char* wg, const char* wu, char* a2f, AS3 char* sm STP_ARG) {
  const int tid = threadIdx.x, lane = tid & 63;
  const int w = __builtin_amdgcn_readfirstlane(tid >> 6);
  const int wm = w >> 2, wn = w & 3;
  const int t0 = mt * MT_MLP, f0 = fs * 192;
  MlpState t;
  const int isUp = wm;
  t.wrs = mkrs((isUp ? wu : wg) + f0 * 2, D_ * F_ * 2 - f0 * 2);
#pragma unroll
  for (int i = 0; i < 3; ++i) {
    int P = (3 * wn + i) * 1024 + lane * 16;
    int row = P / 384, rem = P - row * 384;
    int su = (rem >> 5) ^ ((row >> 1) & 1) ^ (isUp << 1);
    t.wv[i] = row * 3456 + su * 32 + (rem & 16) - i * 1024;
  }
  t.wdst = (u32)(size_t)sm + (isUp ? M_UR : M_GR) + wn * 3072;
  t.xrs = mkrs(x, S_ * D_ * 2);
#pragma unroll
  for (int i = 0; i < 2; ++i) {
    int row = 16 * w + 8 * i + (lane >> 3);
    int tok = min(t0 + row, S_ - 1);
    t.xv[i] = tok * 1280 + (((lane & 7) ^ ((row >> 1) & 7)) << 4) - i * 1024;
  }
  t.xdst = (u32)(size_t)sm + M_XR + w * 2048;
  t.xmask = w == 7 ? 0x00ffffffffffffffull : ~0ull;
  mlp_dma_x<0, 0>(t); mlp_dma_x<0, 1>(t);
  mlp_dma_w<0, 0>(t); mlp_dma_w<0, 1>(t); mlp_dma_w<0, 2>(t);
  mlp_dma_w<1, 0>(t); mlp_dma_w<1, 1>(t); mlp_dma_w<1, 2>(t);
  mlp_dma_x<1, 0>(t); mlp_dma_x<1, 1>(t);
  mlp_dma_w<2, 0>(t); mlp_dma_w<2, 1>(t); mlp_dma_w<2, 2>(t);
  mlp_dma_w<3, 0>(t); mlp_dma_w<3, 1>(t); mlp_dma_w<3, 2>(t);
  mlp_dma_x<2, 0>(t); mlp_dma_x<2, 1>(t);
  sched();
  if (w == 7 && lane < 24) {
    u32x4 ones = {0x3f803f80u, 0x3f803f80u, 0x3f803f80u, 0x3f803f80u};
    *(AS3 u32x4*)(sm + M_XR + (lane >> 3) * 16384 + 127 * 128 + (lane & 7) * 16) = ones;
  }
  {
    int G = lane >> 4, q = (lane >> 2) & 3, p = lane & 3;
    int up = p >> 1;
    int row = 8 * (G >> 1) + q;
#pragma unroll
    for (int b = 0; b < 3; ++b) {
      int su = (wn * 3 + b) ^ ((q >> 1) & 1) ^ (up << 1);
      t.wa[b] = opq((up ? M_UR : M_GR) + row * 384 + su * 32 + (G & 1) * 16 + (p & 1) * 8);
    }
    int xr = wm * 64 + (lane & 31);
#pragma unroll
    for (int c = 0; c < 4; ++c) t.xa[c] = opq(M_XR + xr * 128 + (((c * 2 + (lane >> 5)) ^ ((xr >> 1) & 7)) << 4));
    t.sa = opq(M_XR + w * 2048 + lane * 16);
  }
#pragma unroll
  for (int i = 0; i < 2; ++i)
#pragma unroll
    for (int b = 0; b < 3; ++b)
#pragma unroll
      for (int r = 0; r < 16; ++r) t.acc[i][b][r] = 0.f;
  wait_lgkm<0>();
  wait_vm<13>();
  bar();
  sched();
  STAMP(0, 1);
  mlp_rdx<0, 0, 0>(t, sm); mlp_rdw<0, 0, 0>(t, sm); mlp_rdw<0, 0, 1>(t, sm); mlp_rdw<0, 0, 2>(t, sm); mlp_rdx<0, 0, 1>(t, sm);
  mlp_stage<0>(t, sm);
  STAMP(0, 2);

#pragma unroll
  for (int i = 0; i < 2; ++i) {
    f32x2 r = stat_fin(t.st[i]);
    if ((lane & 7) == 0) *(AS3 f32x2*)(sm + M_ST + (16 * w + 8 * i + (lane >> 3)) * 8) = r;
  }
  const int hh = lane >> 5;
  if (wm == 1 && (lane & 31) == 31) {
#pragma unroll
    for (int b = 0; b < 3; ++b)
#pragma unroll
      for (int g = 0; g < 4; ++g) {
        f32x4 v = {t.acc[1][b][4 * g], t.acc[1][b][4 * g + 1], t.acc[1][b][4 * g + 2], t.acc[1][b][4 * g + 3]};
        *(AS3 f32x4*)(sm + M_CS + ((wn * 3 + b) * 32 + 8 * g + 4 * hh) * 4) = v;
      }
  }
  wait_lgkm<0>();
  bar();
  f32x2 ms[2];
#pragma unroll
  for (int i = 0; i < 2; ++i) ms[i] = *(AS3 f32x2*)(sm + M_ST + (wm * 64 + i * 32 + (lane & 31)) * 8);
#pragma unroll
  for (int b = 0; b < 3; ++b) {
    f32x4 cs[4];
#pragma unroll
    for (int g = 0; g < 4; ++g) cs[g] = *(AS3 f32x4*)(sm + M_CS + ((wn * 3 + b) * 32 + 8 * g + 4 * hh) * 4);
    const int F16 = fs * 12 + wn * 3 + b;
    const int kb = 20 + (F16 >> 1), go = (F16 & 1) * 2 + hh;
#pragma unroll
    for (int i = 0; i < 2; ++i) {
      const float nm = -ms[i][0], rs = ms[i][1];
      float h[8];
#pragma unroll
      for (int e = 0; e < 8; ++e) {
        const int gg = (e >> 2) * 2, r = e & 3;
        float g = fmaf(nm, cs[gg][r], t.acc[i][b][4 * gg + r]) * rs;
        float u = fmaf(nm, cs[gg + 1][r], t.acc[i][b][4 * gg + 4 + r]) * rs;
        float z = g * fmaf(g * g, -0.10294324f, -2.30220820f);
        float sg = __builtin_amdgcn_rcpf(1.f + __builtin_amdgcn_exp2f(z));
        h[e] = g * u * sg;
      }
      const int row = wm * 64 + i * 32 + (lane & 31);
      const int tok = t0 + row;
      if (row < MT_MLP && tok < S_) {
        u32x4 o = {pk2(h[0], h[1]), pk2(h[2], h[3]), pk2(h[4], h[5]), pk2(h[6], h[7])};
        *(u32x4*)(a2f + ((size_t)((tok >> 6) * KB2 + kb) * 4096 + ((tok >> 4) & 3) * 1024 + go * 256 + (tok & 15) * 16)) = o;
      }
    }
  }
}

constexpr int A_WR = 0, A_XR = 65536, A_ST = A_XR + 3 * 24576;
constexpr int att_nd(int s, int a) {
  if (a == 1) return (s + 4 < 20) ? 2 : 0;
  if (s >= 2 && (s - 2) / 2 + 3 < 10) return (s & 1) ? 1 : 2;
  return 0;
}
constexpr int att_vm(int s) {
  int n = att_nd(s, 0);
  if (s >= 1) n += att_nd(s - 1, 0) + att_nd(s - 1, 1);
  if (s >= 2) n += att_nd(s - 2, 1);
  if (s == 0) n += 3 + 2 + 2 + 3;
  if (s == 1) n += 2 + 3;
  if (s == 2) n += 3;
  return n;
}
struct AttState {
  f32x4 acc[6][4];
  bf16x8 wf[2][4], xf[2][3];
  RowStat st[3];
  u32 wv[2], xv[3];
  u32 wa[4], xa[2], sa;
  rsrc_t wrs, xrs;
  u32 wdst, xdst;
  uint64_t xmask;
};
template <int SST, int A, int I> DEVI void att_rdx(AttState& t, AS3 char* sm) {
  t.xf[A][I] = ld128(sm + t.xa[SST & 1] + ((SST >> 1) % 3) * 24576 + A * 6144 + I * 2048);
}
template <int SST, int C> DEVI void att_rdw(AttState& t, AS3 char* sm) {
  constexpr int wo = A_WR + (SST & 3) * 16384;
  t.wf[SST & 1][C] = cat(ldtr(sm + t.wa[C] + wo), ldtr(sm + t.wa[C] + wo + 2048));
}
template <int WS, int I> DEVI void att_dma_w(AttState& t) {
  dma16<WS * 32 * 3840, I * 1024>(t.wrs, t.wv[I], t.wdst + (WS & 3) * 16384);
}
template <int XS, int I> DEVI void att_dma_x(AttState& t) {
  if constexpr (I != 2) dma16<XS * 128, I * 1024>(t.xrs, t.xv[I], t.xdst + (XS % 3) * 24576);
  else dma16m<XS * 128, 2048>(t.xrs, t.xv[2], t.xdst + (XS % 3) * 24576, t.xmask);
}
template <int SST, int A, int I, int C2> DEVI void att_mm(AttState& t) {
  sched();
  t.acc[A * 3 + I][C2 * 2] = __builtin_amdgcn_mfma_f32_16x16x32_bf16(t.wf[SST & 1][C2 * 2], t.xf[A][I], t.acc[A * 3 + I][C2 * 2], 0, 0, 0);
  t.acc[A * 3 + I][C2 * 2 + 1] = __builtin_amdgcn_mfma_f32_16x16x32_bf16(t.wf[SST & 1][C2 * 2 + 1], t.xf[A][I], t.acc[A * 3 + I][C2 * 2 + 1], 0, 0, 0);
  sched();
}
template <int SST> DEVI void att_stage(AttState& t, AS3 char* sm) {
  constexpr int so = ((SST >> 1) % 3) * 24576;
  constexpr bool XD = SST >= 2 && (SST - 2) / 2 + 3 < 10;
  constexpr int XS = (SST - 2) / 2 + 3;
  constexpr bool EV = (SST & 1) == 0;
  att_mm<SST, 0, 0, 0>(t);
  att_rdx<SST, 1, 0>(t, sm); att_rdx<SST, 1, 1>(t, sm);
  att_mm<SST, 0, 0, 1>(t);
  att_rdx<SST, 1, 2>(t, sm);
  bf16x8 sv0 = ld128(sm + t.sa + so + (SST & 1) * 2048), sv1;
  att_mm<SST, 0, 1, 0>(t);
  if constexpr (EV) sv1 = ld128(sm + t.sa + so + 1024);
  att_mm<SST, 0, 1, 1>(t);
  if constexpr (XD) att_dma_x<XS, EV ? 0 : 2>(t);
  att_mm<SST, 0, 2, 0>(t);
  if constexpr (XD && EV) att_dma_x<XS, 1>(t);
  att_mm<SST, 0, 2, 1>(t);
  if constexpr (SST < 19) {
    wait_lgkm<0>();
    wait_vm<att_vm(SST)>();
    bar();
  }
  att_mm<SST, 1, 0, 0>(t);
  if constexpr (SST < 19) { att_rdx<SST + 1, 0, 0>(t, sm); att_rdw<SST + 1, 0>(t, sm); att_rdw<SST + 1, 1>(t, sm); }
  stat_acc<(SST < 2), 0>(t.st[(SST & 1) * 2], sv0);
  att_mm<SST, 1, 0, 1>(t);
  if constexpr (SST < 19) { att_rdw<SST + 1, 2>(t, sm); att_rdw<SST + 1, 3>(t, sm); }
  stat_acc<(SST < 2), 1>(t.st[(SST & 1) * 2], sv0);
  att_mm<SST, 1, 1, 0>(t);
  if constexpr (SST < 19) { att_rdx<SST + 1, 0, 1>(t, sm); att_rdx<SST + 1, 0, 2>(t, sm); }
  stat_acc<(SST < 2), 2>(t.st[(SST & 1) * 2], sv0);
  if constexpr (EV) stat_acc<(SST < 2), 0>(t.st[1], sv1);
  att_mm<SST, 1, 1, 1>(t);
  if constexpr (SST + 4 < 20) att_dma_w<SST + 4, 0>(t);
  stat_acc<(SST < 2), 3>(t.st[(SST & 1) * 2], sv0);
  if constexpr (EV) stat_acc<(SST < 2), 1>(t.st[1], sv1);
  att_mm<SST, 1, 2, 0>(t);
  if constexpr (SST + 4 < 20) att_dma_w<SST + 4, 1>(t);
  if constexpr (EV) stat_acc<(SST < 2), 2>(t.st[1], sv1);
  att_mm<SST, 1, 2, 1>(t);
  if constexpr (EV) stat_acc<(SST < 2), 3>(t.st[1], sv1);
  if constexpr (SST < 19) att_stage<SST + 1>(t, sm);
}

DEVI void k1_att(int mt, int hg, const char* x, const char* wqkv, char* a2f, AS3 char* sm STP_ARG) {
  const int tid = threadIdx.x, lane = tid & 63;
  const int w = __builtin_amdgcn_readfirstlane(tid >> 6);
  const int hl = w & 3, par = w >> 2;
  const int u0 = mt * UT_ATT - 7;
  AttState t;
  t.wrs = mkrs(wqkv + hg * 160, D_ * 3 * D_ * 2 - hg * 160);
#pragma unroll
  for (int i = 0; i < 2; ++i) {
    int row = 4 * w + 2 * i + (lane >> 5);
    int rho = (row & 3) + 4 * ((row >> 3) & 1);
    int c = ((lane & 31) - 2 * rho) & 31;
    int so = c < 10 ? 2560 + c * 16 : (c == 10 || c == 31) ? 0 : c <= 20 ? (c - 11) * 16 : 1280 + (c - 21) * 16;
    t.wv[i] = row * 3840 + so - i * 1024;
  }
  t.wdst = (u32)(size_t)sm + A_WR + w * 2048;
  t.xrs = mkrs(x - 2048, S_ * D_ * 2 + 2048);
#pragma unroll
  for (int i = 0; i < 3; ++i) {
    int row = 24 * w + 8 * i + (lane >> 3);
    int pr = row >= 96 ? 1 : 0;
    int u = min(max(u0 + row - pr * 96, 0), 1023);
    t.xv[i] = (2 * u + pr) * 1280 + (((lane & 7) ^ ((row >> 1) & 7)) << 4) + 2048 - i * 1024;
  }
  t.xdst = (u32)(size_t)sm + A_XR + w * 3072;
  t.xmask = hl == 3 ? 0x00ffffffffffffffull : ~0ull;
  att_dma_x<0, 0>(t); att_dma_x<0, 1>(t); att_dma_x<0, 2>(t);
  att_dma_w<0, 0>(t); att_dma_w<0, 1>(t);
  att_dma_w<1, 0>(t); att_dma_w<1, 1>(t);
  att_dma_x<1, 0>(t); att_dma_x<1, 1>(t); att_dma_x<1, 2>(t);
  att_dma_w<2, 0>(t); att_dma_w<2, 1>(t);
  att_dma_w<3, 0>(t); att_dma_w<3, 1>(t);
  att_dma_x<2, 0>(t); att_dma_x<2, 1>(t); att_dma_x<2, 2>(t);
  sched();
  if (hl == 3 && lane < 24) {
    u32x4 ones = {0x3f803f80u, 0x3f803f80u, 0x3f803f80u, 0x3f803f80u};
    *(AS3 u32x4*)(sm + A_XR + (lane >> 3) * 24576 + (par * 96 + 95) * 128 + (lane & 7) * 16) = ones;
  }
  const int G = lane >> 4, tq = lane & 15;
  {
    int q = (lane >> 2) & 3, p = lane & 3;
    int rho = q + 4 * (G & 1);
    int rowb = (8 * G + q) * 512;
    int hb = hl * 40;
    int o0 = 176 + 8 * p, o1 = 336 + 8 * p;
    int o2 = p == 0 ? 176 + 32 : p == 1 ? 0 : p == 2 ? 8 : 24;
    int o3 = p == 0 ? 336 + 32 : p == 1 ? 32 : p == 2 ? 16 : 8;
    t.wa[0] = opq(rowb + ((o0 + hb + 32 * rho) & 511));
    t.wa[1] = opq(rowb + ((o1 + hb + 32 * rho) & 511));
    t.wa[2] = opq(rowb + ((o2 + hb + 32 * rho) & 511));
    t.wa[3] = opq(rowb + ((o3 + hb + 32 * rho) & 511));
    int xr = par * 96 + tq;
    t.xa[0] = opq(A_XR + xr * 128 + ((G ^ (tq >> 1)) << 4));
    t.xa[1] = opq(t.xa[0] ^ 64);
    t.sa = opq(A_XR + w * 3072 + lane * 16);
  }
#pragma unroll
  for (int i = 0; i < 6; ++i)
#pragma unroll
    for (int c = 0; c < 4; ++c) t.acc[i][c] = f32x4{0.f, 0.f, 0.f, 0.f};
  wait_lgkm<0>();
  wait_vm<12>();
  bar();
  sched();
  STAMP(0, 1);
  att_rdx<0, 0, 0>(t, sm); att_rdw<0, 0>(t, sm); att_rdw<0, 1>(t, sm); att_rdw<0, 2>(t, sm); att_rdw<0, 3>(t, sm);
  att_rdx<0, 0, 1>(t, sm); att_rdx<0, 0, 2>(t, sm);
  att_stage<0>(t, sm);
  STAMP(0, 2);

#pragma unroll
  for (int i = 0; i < 3; ++i) {
    f32x2 r = stat_fin(t.st[i]);
    if ((lane & 7) == 0) *(AS3 f32x2*)(sm + A_ST + (24 * w + 8 * i + (lane >> 3)) * 8) = r;
  }
  f32x4 cs[4];
#pragma unroll
  for (int c = 0; c < 4; ++c)
#pragma unroll
    for (int r = 0; r < 4; ++r) cs[c][r] = dppf<0x15F>(t.acc[5][c][r]);
  wait_lgkm<0>();
  bar();
  AS3 char* vs = sm + A_WR + w * 6144;
  const float qs = 0.22360679775f * 1.44269504089f;
  u32x4 qf[6], kf[6];
#pragma unroll
  for (int T = 0; T < 6; ++T) {
    f32x2 ms = *(AS3 f32x2*)(sm + A_ST + (par * 96 + 16 * T + tq) * 8);
    const float nm = -ms[0], rs = ms[1], rq = rs * qs, r2 = G == 0 ? rq : rs;
    float v[4][4];
#pragma unroll
    for (int c = 0; c < 4; ++c)
#pragma unroll
      for (int r = 0; r < 4; ++r)
        v[c][r] = fmaf(nm, cs[c][r], t.acc[T][c][r]) * (c == 0 ? rq : c == 2 ? r2 : rs);
    u32 c20 = pk2(v[2][0], v[2][1]), c21 = pk2(v[2][2], v[2][3]);
    u32 c30 = pk2(v[3][0], v[3][1]), c31 = pk2(v[3][2], v[3][3]);
    qf[T] = u32x4{pk2(v[0][0], v[0][1]), pk2(v[0][2], v[0][3]), G == 0 ? c20 : 0u, G == 0 ? c21 : 0u};
    kf[T] = u32x4{pk2(v[1][0], v[1][1]), pk2(v[1][2], v[1][3]), G == 0 ? c30 : 0u, G == 0 ? c31 : 0u};
    *(AS3 u32x4*)(vs + (16 * T + tq) * 64 + ((G ^ ((tq >> 1) & 3)) << 4)) = u32x4{c20, c21, c30, c31};
  }
  int va0, va1;
  {
    int q = (lane >> 2) & 3, p = lane & 3;
    int row = 4 * G + q;
    int g0 = p == 0 ? 1 : p == 3 ? 3 : 2, h0 = p == 2 ? 8 : 0;
    va0 = row * 64 + ((g0 ^ ((row >> 1) & 3)) << 4) + h0;
    va1 = row * 64 + ((1 ^ ((row >> 1) & 3)) << 4) + 8;
  }
  const int H = hg * 4 + hl;
  s16x4 vp0 = {0, 0, 0, 0}, vp1 = {0, 0, 0, 0};
#pragma unroll
  for (int T = 0; T < 6; ++T) {
    s16x4 vc0 = ldtr(vs + va0 + T * 1024), vc1 = ldtr(vs + va1 + T * 1024);
    const f32x4 z4 = {0.f, 0.f, 0.f, 0.f};
    f32x4 sa = __builtin_amdgcn_mfma_f32_16x16x32_bf16(__builtin_bit_cast(bf16x8, kf[T]), __builtin_bit_cast(bf16x8, qf[T]), z4, 0, 0, 0);
    f32x4 sb = z4;
    if (T > 0) sb = __builtin_amdgcn_mfma_f32_16x16x32_bf16(__builtin_bit_cast(bf16x8, kf[T > 0 ? T - 1 : 0]), __builtin_bit_cast(bf16x8, qf[T]), z4, 0, 0, 0);
    const float NEG = -__builtin_inff();
    float mx = NEG;
#pragma unroll
    for (int r = 0; r < 4; ++r) {
      int key = 4 * G + r;
      bool oka = key <= tq && key >= tq - 7 && (u0 + 16 * T + key >= 0 || key == tq);
      bool okb = T > 0 && key >= tq + 9 && (u0 + 16 * (T - 1) + key >= 0);
      sa[r] = oka ? sa[r] : NEG;
      sb[r] = okb ? sb[r] : NEG;
      mx = fmaxf(mx, fmaxf(sa[r], sb[r]));
    }
    mx = xg_max(mx);
    float sum = 0.f;
#pragma unroll
    for (int r = 0; r < 4; ++r) {
      sa[r] = __builtin_amdgcn_exp2f(sa[r] - mx);
      sb[r] = __builtin_amdgcn_exp2f(sb[r] - mx);
      sum += sa[r] + sb[r];
    }
    sum = xg_sum(sum);
    const float inv = __builtin_amdgcn_rcpf(sum);
    u32x4 pf = {pk2(sa[0] * inv, sa[1] * inv), pk2(sa[2] * inv, sa[3] * inv), pk2(sb[0] * inv, sb[1] * inv), pk2(sb[2] * inv, sb[3] * inv)};
    f32x4 o0 = __builtin_amdgcn_mfma_f32_16x16x32_bf16(cat(vc0, vp0), __builtin_bit_cast(bf16x8, pf), z4, 0, 0, 0);
    f32x4 o1 = __builtin_amdgcn_mfma_f32_16x16x32_bf16(cat(vc1, vp1), __builtin_bit_cast(bf16x8, pf), z4, 0, 0, 0);
    vp0 = vc0; vp1 = vc1;
    const int i = 16 * T + tq, u = u0 + i;
    if (i >= 7 && i < 7 + UT_ATT && u < 1024) {
      const int tok = 2 * u + par;
      char* base = a2f + (size_t)(tok >> 6) * KB2 * 4096 + ((tok >> 4) & 3) * 1024 + (tok & 15) * 16;
      const int hA = H * 5 + G;
      *(u32x2*)(base + (hA >> 3) * 4096 + ((hA >> 1) & 3) * 256 + (hA & 1) * 8) = u32x2{pk2(o0[0], o0[1]), pk2(o0[2], o0[3])};
      if (G == 0) {
        const int hB = H * 5 + 4;
        *(u32x2*)(base + (hB >> 3) * 4096 + ((hB >> 1) & 3) * 256 + (hB & 1) * 8) = u32x2{pk2(o1[0], o1[1]), pk2(o1[2], o1[3])};
      }
    }
  }
}

DEVI void k1_retile(int id, int nid, const char* wo, const char* wd, char* w2f, AS3 char* sm) {
  const int tid = threadIdx.x, lane = tid & 63;
  const int w = __builtin_amdgcn_readfirstlane(tid >> 6);
  const int G = lane >> 4, q = (lane >> 2) & 3, p = lane & 3;
  const rsrc_t rso = mkrs(wo, D_ * D_ * 2), rsd = mkrs(wd, F_ * D_ * 2);
  const u32 vo = w * 5120 + lane * 16;
  auto issue = [&](int kb, int slot) {
    const rsrc_t rs = kb < 20 ? rso : rsd;
    const u32 so = __builtin_amdgcn_readfirstlane((kb < 20 ? kb : kb - 20) * 40960), ld = __builtin_amdgcn_readfirstlane((u32)(size_t)sm + slot * 40960 + w * 5120);
    dma16s<0>(rs, vo, so, ld); dma16s<1024>(rs, vo, so, ld); dma16s<2048>(rs, vo, so, ld); dma16s<3072>(rs, vo, so, ld);
    dma16s<0>(rs, vo, so + 4096, ld + 4096);
  };
  const int n = (KB2 - id + nid - 1) / nid;
  issue(id, 0);
  issue(min(id + nid, KB2 - 1), 1);
  int slot = 0;
  for (int it = 0; it < n; ++it) {
    const int kb = id + it * nid;
    if (it == 0) wait_vm<5>(); else wait_vm<10>();
    bar();
    int s2 = slot + 2; if (s2 >= 3) s2 -= 3;
    issue(min(kb + 2 * nid, KB2 - 1), s2);
    const int r0 = kb < 20 ? 8 * G + q : (G >> 1) * 16 + (G & 1) * 4 + q;
    const int rd = kb < 20 ? 4 : 8;
    AS3 char* b = sm + slot * 40960 + r0 * 1280 + w * 160;
    u32x4 o[5];
#pragma unroll
    for (int nb = 0; nb < 5; ++nb) {
      int cb = nb < 4 ? (nb >> 1) * 64 + 16 * p + 8 * (nb & 1) : 128 + 8 * p;
      o[nb] = __builtin_bit_cast(u32x4, cat(ldtr(b + cb), ldtr(b + cb + rd * 1280)));
    }
#pragma unroll
    for (int nb = 0; nb < 5; ++nb) *(u32x4*)(w2f + (size_t)((w * KB2 + kb) * 5 + nb) * 1024 + lane * 16) = o[nb];
    slot = slot + 1 >= 3 ? 0 : slot + 1;
  }
  wait_vm<0>();
}

__global__ __launch_bounds__(512, 2) void k1(const char* __restrict__ x, const char* __restrict__ wqkv,
                                             const char* __restrict__ wo, const char* __restrict__ wg,
                                             const char* __restrict__ wu, const char* __restrict__ wd,
                                             char* __restrict__ a2f, char* __restrict__ w2f) {
  __shared__ __attribute__((aligned(1024))) char smem[LDS_K1];
  AS3 char* sm = (AS3 char*)smem;
  const int b = blockIdx.x, xcd = b & 7, idx = b >> 3;
  const int xm = xcd >> 2, xn = xcd & 3;
  STP_DECL;
  STAMP(0, 0);
  if (idx < 12) {
    k1_att(xm * 6 + (idx >> 1), xn * 2 + (idx & 1), x, wqkv, a2f, sm STP_PASS);
    STAMP(0, 3);
    return;
  }
  const int j = idx - 12;
  int mt, fs;
  if (xm == 0) {
    const int q = xn * 20 + j;
    mt = q % 9; fs = q / 9;
  } else {
    const int nt = xn == 3 ? 19 : 18;
    if (j >= nt) {
      k1_retile(xn * 2 + (j - nt), 7, wo, wd, w2f, sm);
      STAMP(0, 3);
      return;
    }
    const int e = xn * 18 + j;
    mt = e < 72 ? 9 + (e & 7) : 8; fs = e < 72 ? e >> 3 : 8;
  }
  k1_mlp(mt, fs, x, wg, wu, a2f, sm STP_PASS);
  STAMP(0, 3);
}

constexpr int K2P = 6;
struct K2State {
  f32x4 acc[4][5];
  bf16x8 a[K2P][4], wv[K2P][5];
  const char* pa; const char* pw;
};
template <int I> DEVI void k2_load(K2State& t) {
#pragma unroll
  for (int i = 0; i < 4; ++i) t.a[I % K2P][i] = *(const bf16x8*)(t.pa + (size_t)I * 16384 + i * 1024);
#pragma unroll
  for (int i = 0; i < 5; ++i) t.wv[I % K2P][i] = *(const bf16x8*)(t.pw + (size_t)I * 20480 + i * 1024);
}
template <int I> DEVI void k2_mfma(K2State& t) {
#pragma unroll
  for (int n = 0; n < 5; ++n)
#pragma unroll
    for (int i = 0; i < 4; ++i)
      t.acc[i][n] = __builtin_amdgcn_mfma_f32_16x16x32_bf16(t.wv[I % K2P][n], t.a[I % K2P][i], t.acc[i][n], 0, 0, 0);
}
template <int I> DEVI void k2_step(K2State& t, bool extra) {
  k2_mfma<I>(t);
  sched();
  if constexpr (I + K2P < 18) k2_load<I + K2P>(t);
  if constexpr (I + K2P == 18) { if (extra) k2_load<18>(t); }
  sched();
  if constexpr (I < 17) k2_step<I + 1>(t, extra);
}
template <int I> DEVI void k2_pro(K2State& t) {
  k2_load<I>(t);
  if constexpr (I + 1 < K2P) k2_pro<I + 1>(t);
}

__global__ __launch_bounds__(256, 1) void k2(const char* __restrict__ a2f, const char* __restrict__ w2f,
                                             const char* __restrict__ x, char* __restrict__ out) {
  __shared__ __attribute__((aligned(1024))) char smem[4 * 4 * 5 * 1024];
  AS3 char* sm = (AS3 char*)smem;
  const int b = blockIdx.x, xcd = b & 7, idx = b >> 3;
  const int mi = (xcd >> 1) * 8 + (idx >> 2), ni = (xcd & 1) * 4 + (idx & 3);
  const int lane = threadIdx.x & 63, w = __builtin_amdgcn_readfirstlane(threadIdx.x >> 6);
  K2State t;
  STP_DECL;
  STAMP(1, 0);
  t.pa = a2f + (size_t)(mi * KB2 + w) * 4096 + lane * 16;
  t.pw = w2f + (size_t)(ni * KB2 + w) * 5120 + lane * 16;
  k2_pro<0>(t);
  const int tok = mi * 64 + w * 16 + (lane & 15), G = lane >> 4;
  const size_t ro = (size_t)tok * 1280 + ni * 160;
  u32x4 xr0 = *(const u32x4*)(x + ro + G * 16), xr1 = *(const u32x4*)(x + ro + 64 + G * 16);
  u32x2 xr2 = *(const u32x2*)(x + ro + 128 + G * 8);
#pragma unroll
  for (int i = 0; i < 4; ++i)
#pragma unroll
    for (int n = 0; n < 5; ++n) t.acc[i][n] = f32x4{0.f, 0.f, 0.f, 0.f};
  sched();
  const bool extra = w < 2;
  STAMP(1, 1);
  k2_step<0>(t, extra);
  if (extra) k2_mfma<18>(t);
  STAMP(1, 2);
#pragma unroll
  for (int i = 0; i < 4; ++i)
#pragma unroll
    for (int n = 0; n < 5; ++n) *(AS3 f32x4*)(sm + ((i * 4 + w) * 5 + n) * 1024 + lane * 16) = t.acc[i][n];
  __syncthreads();
  f32x4 r[5];
#pragma unroll
  for (int n = 0; n < 5; ++n) {
    r[n] = *(AS3 f32x4*)(sm + ((w * 4 + 0) * 5 + n) * 1024 + lane * 16);
#pragma unroll
    for (int s = 1; s < 4; ++s) r[n] += *(AS3 f32x4*)(sm + ((w * 4 + s) * 5 + n) * 1024 + lane * 16);
  }
  u32x4 o0 = {pk2(r[0][0] + lo16(xr0[0]), r[0][1] + hi16(xr0[0])), pk2(r[0][2] + lo16(xr0[1]), r[0][3] + hi16(xr0[1])),
              pk2(r[1][0] + lo16(xr0[2]), r[1][1] + hi16(xr0[2])), pk2(r[1][2] + lo16(xr0[3]), r[1][3] + hi16(xr0[3]))};
  u32x4 o1 = {pk2(r[2][0] + lo16(xr1[0]), r[2][1] + hi16(xr1[0])), pk2(r[2][2] + lo16(xr1[1]), r[2][3] + hi16(xr1[1])),
              pk2(r[3][0] + lo16(xr1[2]), r[3][1] + hi16(xr1[2])), pk2(r[3][2] + lo16(xr1[3]), r[3][3] + hi16(xr1[3]))};
  u32x2 o2 = {pk2(r[4][0] + lo16(xr2[0]), r[4][1] + hi16(xr2[0])), pk2(r[4][2] + lo16(xr2[1]), r[4][3] + hi16(xr2[1]))};
  *(u32x4*)(out + ro + G * 16) = o0;
  *(u32x4*)(out + ro + 64 + G * 16) = o1;
  *(u32x2*)(out + ro + 128 + G * 8) = o2;
  STAMP(1, 3);
}

extern "C" void kernel_launch(void* const* d_in, const int* in_sizes, int n_in, void* d_out, int out_size,
                              void* d_ws, size_t ws_size, hipStream_t stream) {
  const char* x = (const char*)d_in[0];
  char* a2f = (char*)d_ws;
  char* w2f = a2f + A2F_BYTES;
  k1<<<256, 512, 0, stream>>>(x, (const char*)d_in[1], (const char*)d_in[2], (const char*)d_in[4],
                              (const char*)d_in[5], (const char*)d_in[6], a2f, w2f);
  k2<<<256, 256, 0, stream>>>(a2f, w2f, x, (char*)d_out);
}
```

```cpp
#include <hip/hip_runtime.h>
#include <stdint.h>

typedef unsigned short u16;
typedef unsigned int u32;
typedef __attribute__((ext_vector_type(8))) __bf16 bf16x8;
typedef __attribute__((ext_vector_type(2))) __bf16 bf16x2;
typedef __attribute__((ext_vector_type(4))) short s16x4;
typedef __attribute__((ext_vector_type(8))) short s16x8;
typedef __attribute__((ext_vector_type(2))) float f32x2;
typedef __attribute__((ext_vector_type(4))) float f32x4;
typedef __attribute__((ext_vector_type(4))) u32 u32x4;
typedef __attribute__((ext_vector_type(2))) u32 u32x2;
#define AS3 __attribute__((address_space(3)))
#define DEVI __device__ __forceinline__

constexpr int S_ = 2048, D_ = 640, F_ = 1728;
constexpr int KB2 = 74;
constexpr int A2F_BYTES = 32 * KB2 * 4096;
constexpr int W2F_BYTES = 8 * KB2 * 5120;
constexpr int HT_MLP = 61;
constexpr int UT_ATT = 86;
#define CPRIO 0
#define LPRIO 3
#define ST_AUX 16

template <int N> DEVI void wait_vm() { asm volatile("s_waitcnt vmcnt(%0)" ::"n"(N) : "memory"); }
template <int N> DEVI void wait_lgkm() { asm volatile("s_waitcnt lgkmcnt(%0)" ::"n"(N) : "memory"); }
DEVI void bar() { __builtin_amdgcn_s_barrier(); }
DEVI void sched() { __builtin_amdgcn_sched_barrier(0); }
DEVI u32 opq(u32 v) { asm volatile("" : "+v"(v)); return v; }

DEVI u32 pk2(float a, float b) {
  f32x2 v = {a, b};
  bf16x2 r = __builtin_convertvector(v, bf16x2);
  return __builtin_bit_cast(u32, r);
}
DEVI u32 pk2v(f32x2 v) { return __builtin_bit_cast(u32, __builtin_convertvector(v, bf16x2)); }
DEVI float rbf(float a) { return __uint_as_float(pk2(a, 0.f) << 16); }
DEVI float lo16(u32 v) { return __uint_as_float(v << 16); }
DEVI float hi16(u32 v) { return __uint_as_float(v & 0xffff0000u); }
DEVI f32x2 bc2(float a) { return f32x2{a, a}; }
DEVI f32x2 fma2(f32x2 a, f32x2 b, f32x2 c) { return __builtin_elementwise_fma(a, b, c); }
template <int I> DEVI f32x2 half2(f32x4 v) { return __builtin_shufflevector(v, v, 2 * I, 2 * I + 1); }

typedef __amdgpu_buffer_rsrc_t rsrc_t;
DEVI rsrc_t mkrs(const char* p, int bytes) { return __builtin_amdgcn_make_buffer_rsrc((void*)p, (short)0, bytes, 0x00020000); }
template <int LOFF> DEVI void dma16(rsrc_t rs, u32 voff, u32 soff, u32 lds) {
  asm volatile("s_add_u32 m0, %0, %4\n\ts_nop 0\n\tbuffer_load_dwordx4 %1, %2, %3 offen lds"
               :: "s"(lds), "v"(voff), "s"(rs), "s"(soff), "i"(LOFF) : "memory", "scc");
}
template <int LOFF> DEVI void dma16m(rsrc_t rs, u32 voff, u32 soff, u32 lds, uint64_t mask) {
  asm volatile("s_add_u32 m0, %0, %4\n\ts_mov_b64 exec, %5\n\tbuffer_load_dwordx4 %1, %2, %3 offen lds\n\ts_mov_b64 exec, -1"
               :: "s"(lds), "v"(voff), "s"(rs), "s"(soff), "i"(LOFF), "s"(mask) : "memory", "scc");
}
DEVI void st128(u32x4 v, rsrc_t rs, u32 voff) { __builtin_amdgcn_raw_buffer_store_b128(v, rs, voff, 0, ST_AUX); }
DEVI void st64(u32x2 v, rsrc_t rs, u32 voff) { __builtin_amdgcn_raw_buffer_store_b64(v, rs, voff, 0, ST_AUX); }
DEVI rsrc_t mkrso(const char* p, int bytes, int off) { return mkrs(p + off, bytes - off); }
DEVI bf16x8 ld128(AS3 char* p) { return *(AS3 bf16x8*)p; }
DEVI s16x4 ldtr(AS3 char* p) { return __builtin_amdgcn_ds_read_tr16_b64_v4i16((AS3 s16x4*)p); }
DEVI bf16x8 cat(s16x4 a, s16x4 b) {
  s16x8 r = __builtin_shufflevector(a, b, 0, 1, 2, 3, 4, 5, 6, 7);
  return __builtin_bit_cast(bf16x8, r);
}
DEVI f32x4 mfma(bf16x8 a, bf16x8 b, f32x4 c) { return __builtin_amdgcn_mfma_f32_16x16x32_bf16(a, b, c, 0, 0, 0); }
DEVI f32x4 mfmau(u32x4 a, u32x4 b, f32x4 c) { return mfma(__builtin_bit_cast(bf16x8, a), __builtin_bit_cast(bf16x8, b), c); }
template <int CTRL> DEVI float dppf(float v) {
  return __int_as_float(__builtin_amdgcn_update_dpp(0, __float_as_int(v), CTRL, 0xf, 0xf, true));
}
DEVI float sum8(float v) {
  v += dppf<0xB1>(v);
  v += dppf<0x4E>(v);
  v += dppf<0x141>(v);
  return v;
}
DEVI float xg_max(float x) {
  auto s = __builtin_amdgcn_permlane16_swap(__float_as_uint(x), __float_as_uint(x), false, false);
  x = fmaxf(__uint_as_float(s[0]), __uint_as_float(s[1]));
  auto t = __builtin_amdgcn_permlane32_swap(__float_as_uint(x), __float_as_uint(x), false, false);
  return fmaxf(__uint_as_float(t[0]), __uint_as_float(t[1]));
}
DEVI u32x4 pair_rows(u32 a0, u32 a1, u32 b0, u32 b1) {
  auto s = __builtin_amdgcn_permlane16_swap(a0, b0, false, false);
  auto t = __builtin_amdgcn_permlane16_swap(a1, b1, false, false);
  return u32x4{s[0], t[0], s[1], t[1]};
}
DEVI int tok_of(int sg) { return (sg & ~31) | ((sg & 15) << 1) | ((sg >> 4) & 1); }
DEVI u32 a2f_off(int sg) { return (sg >> 6) * (KB2 * 4096) + ((sg >> 4) & 3) * 1024 + (sg & 15) * 16; }
constexpr u32 OOB = 0x80000000u;

#define CAP 5
#define PLO 9
#define PHI 22
#define RINT 8
#define PFW0 0
#define PFW1 3
#define PFW2 7
#define PFX0 0
#define PFX1 4
enum { T_X, T_W, T_R, T_S, T_REC, T_PW, T_PX };
struct Sched { int n, ty[128], idx[128], sub[128], first[23], vm[21]; };
constexpr Sched mk_sched(int NW, int NX, int NR) {
  Sched s = {};
  int al[128] = {}, du[128] = {}, ty[128] = {}, ix[128] = {}, sb[128] = {}, n = 0;
  for (int j = 0; j < 10; ++j) {
    for (int p = 0; p < NX; ++p) { ty[n] = T_X; ix[n] = j; sb[n] = p; al[n] = j < 3 ? -1 : 2 * j - 4; du[n] = 2 * j - 1; ++n; }
    for (int i = 2 * j; i < 2 * j + 2; ++i)
      for (int p = 0; p < NW; ++p) { ty[n] = T_W; ix[n] = i; sb[n] = p; al[n] = i < 4 ? -1 : i - 3; du[n] = i - 1; ++n; }
  }
  int due[128] = {}, q = 0;
  for (int m = -1; m <= 20; ++m) {
    s.first[m + 1] = s.n;
    int c = 0;
    while (q < n && c < (m < 0 ? NX + 2 * NW : CAP) && al[q] <= m) {
      s.ty[s.n] = ty[q]; s.idx[s.n] = ix[q]; s.sub[s.n] = sb[q]; due[s.n] = du[q]; ++s.n; ++q; ++c;
    }
    if (m == -1) { s.ty[s.n] = T_S; due[s.n] = 99; ++s.n; }
    if (m == PFW0 || m == PFW1 || m == PFW2) { s.ty[s.n] = T_PW; s.sub[s.n] = m == PFW0 ? 0 : m == PFW1 ? 192 : 320; due[s.n] = 99; ++s.n; }
    if (m == PFX0 || m == PFX1) { s.ty[s.n] = T_PX; s.sub[s.n] = m == PFX0 ? 0 : 384; due[s.n] = 99; ++s.n; }
    if (m == RINT) for (int p = 0; p < 2; ++p) { s.ty[s.n] = T_R; s.sub[s.n] = p; due[s.n] = 18; ++s.n; }
    if (m == 17) for (int p = 0; p < NR; ++p) { s.ty[s.n] = T_REC; s.sub[s.n] = p; due[s.n] = 99; ++s.n; }
    if (m <= 19) {
      int need = 0;
      for (int k = 0; k < s.n; ++k) if (due[k] <= m) need = k + 1;
      s.vm[m + 1] = s.n - need;
    }
  }
  s.first[22] = s.n;
  return s;
}
template <bool ATT> struct Cfg {
  static constexpr int NW = ATT ? 2 : 3, NX = ATT ? 3 : 2;
  static constexpr int NR = ATT ? 6 : 4;
  static constexpr int WSLOT = NW * 8192, XSLOT = NX * 8192;
  static constexpr int XR = 4 * WSLOT, RT = XR + 3 * XSLOT;
  static constexpr int WSTEP = 32 * (ATT ? 3 * D_ * 2 : F_ * 2);
  static constexpr Sched SC = mk_sched(NW, NX, NR);
  static constexpr int vm(int m) { return SC.vm[m + 1]; }
  static constexpr int at(int m, int r) {
    const int f = SC.first[m + 1], c = SC.first[m + 2] - f;
    const int lo = m == 0 ? 10 : c > 4 ? 1 : PLO;
    for (int k = 0; k < c; ++k) if (lo + (c > 1 ? k * (PHI - lo) / (c - 1) : 0) == r) return f + k;
    return -1;
  }
};
constexpr int LDS_K1 = 163840;
constexpr u32 TAGC = 0x9E3779B9u;

struct StatLd { u32x4 a; u32 b, e; };
DEVI void stat_load(StatLd& s, rsrc_t xrs, rsrc_t srs, int tok, int lane) {
  asm volatile("buffer_load_dwordx4 %0, %3, %5, %6 offen\n\tbuffer_load_dword %1, %4, %5, %6 offen offset:1024\n\t"
               "buffer_load_dword %2, off, %7, %8"
               : "=&v"(s.a), "=&v"(s.b), "=&v"(s.e)
               : "v"(lane * 16), "v"(lane * 4), "s"(xrs), "s"(tok * 1280), "s"(srs), "s"(S_ * 8) : "memory");
}
DEVI float wsum(float v) {
  v = sum8(v);
  v += dppf<0x140>(v);
  auto s = __builtin_amdgcn_permlane16_swap(__float_as_uint(v), __float_as_uint(v), false, false);
  v = __uint_as_float(s[0]) + __uint_as_float(s[1]);
  auto t = __builtin_amdgcn_permlane32_swap(__float_as_uint(v), __float_as_uint(v), false, false);
  return __uint_as_float(t[0]) + __uint_as_float(t[1]);
}
template <int N> DEVI u32 stat_pub(StatLd& s, rsrc_t srs, int tok, int lane) {
  asm volatile("s_waitcnt vmcnt(%3)" : "+v"(s.a), "+v"(s.b), "+v"(s.e) : "n"(N) : "memory");
  const u32 tag = s.e + TAGC;
  f32x2 e[5];
#pragma unroll
  for (int i = 0; i < 4; ++i) e[i] = f32x2{lo16(s.a[i]), hi16(s.a[i])};
  e[4] = f32x2{lo16(s.b), hi16(s.b)};
  f32x2 a = (e[0] + e[1]) + (e[2] + e[3]) + e[4];
  const float mean = wsum(a[0] + a[1]) * (1.f / 640.f);
  f32x2 q = bc2(0.f);
#pragma unroll
  for (int i = 0; i < 5; ++i) { const f32x2 d = e[i] - bc2(mean); q = fma2(d, d, q); }
  const float var = wsum(q[0] + q[1]) * (1.f / 640.f);
  const float den = __builtin_amdgcn_sqrtf(rbf(rbf(var) + 1e-5f));
  st64(u32x2{pk2(mean, den), tag}, srs, lane == 0 ? tok * 8 : OOB);
  return tag;
}
DEVI void rec_load(u32x2& r, rsrc_t srs, u32 voff) {
  asm volatile("buffer_load_dwordx2 %0, %1, %2, 0 offen sc1" : "=v"(r) : "v"(voff), "s"(srs) : "memory");
}

DEVI void touch(u32& d, rsrc_t rs, u32 voff, u32 soff) {
  asm volatile("buffer_load_dword %0, %1, %2, %3 offen" : "+v"(d) : "v"(voff), "s"(rs), "s"(soff) : "memory");
}

template <bool ATT> struct CS {
  f32x4 acc[6][4];
  bf16x8 S[4], H[2][4];
  u32 wa[4], xa[2];
  u32 wv[Cfg<ATT>::NW], xv[Cfg<ATT>::NX], ro[Cfg<ATT>::NR];
  u32x2 rec[Cfg<ATT>::NR];
  s16x4 rt[4];
  rsrc_t wrs, xrs, srs, ors, rrs;
  u32 pw, px, dummy, rv[2], rdst, wdst, xdst, ra, rso, rso2, tag;
  uint64_t xmask, bad;
};
template <bool ATT, int I, int P> DEVI void dma_w(CS<ATT>& t) {
  using C = Cfg<ATT>;
  dma16<(I & 3) * C::WSLOT + P * 1024>(t.wrs, t.wv[P], I * C::WSTEP, t.wdst);
}
template <bool ATT, int I, int P> DEVI void dma_x(CS<ATT>& t) {
  using C = Cfg<ATT>;
  if constexpr (P + 1 < C::NX) dma16<(I % 3) * C::XSLOT + P * 1024>(t.xrs, t.xv[P], I * 128, t.xdst);
  else dma16m<(I % 3) * C::XSLOT + P * 1024>(t.xrs, t.xv[P], I * 128, t.xdst, t.xmask);
}
template <bool ATT, int I> DEVI void dma_wall(CS<ATT>& t) {
  dma_w<ATT, I, 0>(t); dma_w<ATT, I, 1>(t);
  if constexpr (!ATT) dma_w<ATT, I, 2>(t);
}
template <bool ATT, int I> DEVI void dma_xall(CS<ATT>& t) {
  dma_x<ATT, I, 0>(t); dma_x<ATT, I, 1>(t);
  if constexpr (ATT) dma_x<ATT, I, 2>(t);
}
template <bool ATT> DEVI void rec_wait(CS<ATT>& t) {
  using C = Cfg<ATT>;
  if constexpr (ATT) asm volatile("s_waitcnt vmcnt(0)" : "+v"(t.rec[0]), "+v"(t.rec[1]), "+v"(t.rec[2]), "+v"(t.rec[3]), "+v"(t.rec[4]), "+v"(t.rec[5]), "+v"(t.dummy) :: "memory");
  else asm volatile("s_waitcnt vmcnt(0)" : "+v"(t.rec[0]), "+v"(t.rec[1]), "+v"(t.rec[2]), "+v"(t.rec[3]), "+v"(t.dummy) :: "memory");
}
template <bool ATT> DEVI void rec_test(CS<ATT>& t) {
  rec_wait<ATT>(t);
  u32 bad = 0;
#pragma unroll
  for (int i = 0; i < Cfg<ATT>::NR; ++i) bad |= t.rec[i][1] ^ t.tag;
  t.bad = __builtin_amdgcn_ballot_w64(bad != 0);
}
template <bool ATT> DEVI void rec_retry(CS<ATT>& t) {
#pragma unroll
  for (int i = 0; i < 6; ++i) asm volatile("" : "+v"(t.acc[i][0]), "+v"(t.acc[i][1]), "+v"(t.acc[i][2]), "+v"(t.acc[i][3]));
  while (__builtin_expect(t.bad != 0, 0)) {
    __builtin_amdgcn_s_sleep(2);
    rec_load(t.rec[0], t.srs, t.ro[0]); rec_load(t.rec[1], t.srs, t.ro[1]);
    rec_load(t.rec[2], t.srs, t.ro[2]); rec_load(t.rec[3], t.srs, t.ro[3]);
    if constexpr (ATT) { rec_load(t.rec[4], t.srs, t.ro[4]); rec_load(t.rec[5], t.srs, t.ro[5]); }
    rec_test<ATT>(t);
  }
}
template <bool ATT, int Q> DEVI void piece(CS<ATT>& t) {
  using C = Cfg<ATT>;
  constexpr int ty = C::SC.ty[Q], I = C::SC.idx[Q], P = C::SC.sub[Q];
  if constexpr (ty == T_X) dma_x<ATT, I, P>(t);
  if constexpr (ty == T_W) dma_w<ATT, I, P>(t);
  if constexpr (ty == T_R) dma16<P * 1024>(t.rrs, t.rv[P], 0, t.rdst);
  if constexpr (ty == T_PW) touch(t.dummy, t.wrs, t.pw, P * (C::WSTEP / 32));
  if constexpr (ty == T_PX) touch(t.dummy, t.xrs, t.px, P);
  if constexpr (ty == T_REC) rec_load(t.rec[P], t.srs, t.ro[P]);
}
template <bool ATT, int ST, int Q> DEVI void hook(CS<ATT>& t, AS3 char* sm) {
  using C = Cfg<ATT>;
  constexpr int M = Q >= 14 ? ST + 1 : ST, R = Q >= 14 ? Q - 14 : Q + 10;
  if constexpr (C::at(M, R) >= 0) piece<ATT, C::at(M, R)>(t);
  if constexpr (M == 19 && R == 11) { t.rt[0] = ldtr(sm + t.ra); t.rt[1] = ldtr(sm + t.ra + 256); }
  if constexpr (M == 19 && R == 13) { t.rt[2] = ldtr(sm + t.ra + 8); t.rt[3] = ldtr(sm + t.ra + 264); }
  if constexpr (M == 19 && R == 19) st128(__builtin_bit_cast(u32x4, cat(t.rt[0], t.rt[1])), t.ors, t.rso);
  if constexpr (M == 19 && R == 21) st128(__builtin_bit_cast(u32x4, cat(t.rt[2], t.rt[3])), t.ors, t.rso2);
}
template <bool ATT, int ST, int I> DEVI bf16x8 rd_w(CS<ATT>& t, AS3 char* sm) {
  if constexpr (ATT) {
    constexpr int o = (ST & 3) * 16384;
    return cat(ldtr(sm + t.wa[I] + o), ldtr(sm + t.wa[I] + o + 2048));
  } else {
    constexpr int o = (ST & 1) * 24576 + (I & 1) * 12288 + (I >> 1) * 128;
    return cat(ldtr(sm + t.wa[(ST >> 1) & 1] + o), ldtr(sm + t.wa[(ST >> 1) & 1] + o + 1536));
  }
}
template <bool ATT, int ST, int I> DEVI bf16x8 rd_x(CS<ATT>& t, AS3 char* sm) {
  return ld128(sm + t.xa[ST & 1] + ((ST >> 1) % 3) * Cfg<ATT>::XSLOT + I * 2048);
}
template <bool ATT, int ST, int I> DEVI void rdS(CS<ATT>& t, AS3 char* sm) {
  if constexpr (ST < 20) {
    if constexpr (ATT) t.S[(6 * ST + I) & 3] = rd_x<ATT, ST, I>(t, sm);
    else t.S[(6 * ST + I) & 3] = rd_w<ATT, ST, I>(t, sm);
  }
}
template <bool ATT, int ST, int I> DEVI void rdH(CS<ATT>& t, AS3 char* sm) {
  if constexpr (ST < 20) {
    if constexpr (ATT) t.H[ST & 1][I] = rd_w<ATT, ST, I>(t, sm);
    else t.H[ST & 1][I] = rd_x<ATT, ST, I>(t, sm);
  }
}
template <bool ATT, int ST, int I, int J> DEVI void mm(CS<ATT>& t, AS3 char* sm) {
  sched();
  {
  if constexpr (ATT) t.acc[I][J] = mfma(t.H[ST & 1][J], t.S[(6 * ST + I) & 3], t.acc[I][J]);
  else t.acc[I][J] = mfma(t.S[(6 * ST + I) & 3], t.H[ST & 1][J], t.acc[I][J]);
  }
  sched();
  hook<ATT, ST, I * 4 + J>(t, sm);
  sched();
}
template <bool A, int ST> DEVI void stage(CS<A>& t, AS3 char* sm) {
  mm<A, ST, 0, 0>(t, sm); rdS<A, ST, 3>(t, sm); mm<A, ST, 0, 1>(t, sm); mm<A, ST, 0, 2>(t, sm); mm<A, ST, 0, 3>(t, sm);
  mm<A, ST, 1, 0>(t, sm); rdS<A, ST, 4>(t, sm); mm<A, ST, 1, 1>(t, sm); mm<A, ST, 1, 2>(t, sm); mm<A, ST, 1, 3>(t, sm);
  mm<A, ST, 2, 0>(t, sm); rdS<A, ST, 5>(t, sm); mm<A, ST, 2, 1>(t, sm); mm<A, ST, 2, 2>(t, sm); mm<A, ST, 2, 3>(t, sm);
  mm<A, ST, 3, 0>(t, sm); mm<A, ST, 3, 1>(t, sm);
  if constexpr (ST < 19) { wait_lgkm<0>(); wait_vm<Cfg<A>::vm(ST)>(); bar(); sched(); }
  rdS<A, ST + 1, 0>(t, sm); mm<A, ST, 3, 2>(t, sm); rdH<A, ST + 1, 0>(t, sm); mm<A, ST, 3, 3>(t, sm); rdH<A, ST + 1, 1>(t, sm);
  mm<A, ST, 4, 0>(t, sm); rdS<A, ST + 1, 1>(t, sm); mm<A, ST, 4, 1>(t, sm); rdH<A, ST + 1, 2>(t, sm);
  mm<A, ST, 4, 2>(t, sm); rdH<A, ST + 1, 3>(t, sm); mm<A, ST, 4, 3>(t, sm);
  mm<A, ST, 5, 0>(t, sm); rdS<A, ST + 1, 2>(t, sm); mm<A, ST, 5, 1>(t, sm); mm<A, ST, 5, 2>(t, sm); mm<A, ST, 5, 3>(t, sm);
  if constexpr (ST == 18) { rec_test<A>(t); sched(); }
  if constexpr (ST < 19) stage<A, ST + 1>(t, sm);
}

struct Ptrs { const char *x, *w0, *w1, *wo, *wd; char *a2f, *w2f, *st; };

template <bool ATT> DEVI void k1_main(CS<ATT>& t, int w, int mt, int ns, const Ptrs& P, AS3 char* sm) {
  using C = Cfg<ATT>;
  const int lane = threadIdx.x & 63, wh = w >> 2, wq = w & 3;
  const int unit = blockIdx.x * 8 + w;
  t.xrs = mkrs(P.x, S_ * D_ * 2);
  t.srs = mkrs(P.st, S_ * 8 + 4);
  StatLd sl;
  stat_load(sl, t.xrs, t.srs, unit, lane);
#pragma unroll
  for (int i = 0; i < C::NX; ++i) {
    const int r = (wq * C::NX + i) * 8 + (lane >> 3);
    int tok;
    if (ATT) tok = 2 * min(max(mt * UT_ATT - 7 + r, 0), 1023) + wh;
    else tok = tok_of(min((2 * mt + wh) * HT_MLP + r, S_ - 1));
    t.xv[i] = tok * 1280 + (((lane & 7) ^ ((r >> 1) & 7)) << 4);
  }
  t.xdst = (u32)(size_t)sm + C::XR + w * (C::NX * 1024);
  t.xmask = wq == 3 ? 0x00ffffffffffffffull : ~0ull;
  dma_xall<ATT, 0>(t);
  if (ATT) {
    t.wrs = mkrs(P.w0 + ns * 160, D_ * 3 * D_ * 2 - ns * 160);
#pragma unroll
    for (int i = 0; i < C::NW; ++i) {
      int row = 4 * w + 2 * i + (lane >> 5);
      int rho = (row & 3) + 4 * ((row >> 3) & 1);
      int c = ((lane & 31) - 2 * rho) & 31;
      int so = c < 10 ? 2560 + c * 16 : (c == 10 || c == 31) ? 0 : c <= 20 ? (c - 11) * 16 : 1280 + (c - 21) * 16;
      t.wv[i] = row * 3840 + so;
    }
  } else {
    t.wrs = mkrs((wh ? P.w1 : P.w0) + ns * 384, D_ * F_ * 2 - ns * 384);
#pragma unroll
    for (int i = 0; i < C::NW; ++i) {
      int Q = i * 64 + lane;
      int r = Q / 24, c = Q - r * 24, row = wq * 8 + r;
      int u = (c >> 1) ^ ((row >> 1) & 1) ^ ((row >> 2) & 2);
      t.wv[i] = row * 3456 + u * 32 + (c & 1) * 16;
    }
  }
  t.wdst = (u32)(size_t)sm + w * (C::NW * 1024);
  dma_wall<ATT, 0>(t); dma_wall<ATT, 1>(t);
  sched();
  {
    const int xcd = blockIdx.x & 7, idx = blockIdx.x >> 3, xn = xcd & 3;
    const int id = ATT ? (idx * 8 + w) * 18 + lane : ((idx - 12) * 4 + wq) * 24 + lane;
    const int r = id / 9, l = id - r * 9;
    const int col = ATT ? ((xn * 320) & ~127) - ns * 160 + (l / 3) * (1280 - 384) + l * 128 : (2 * xn - ns) * 384 + l * 128;
    t.pw = (lane < (ATT ? 18 : 24) && r < 192) ? (128 + r) * (C::WSTEP / 32) + col : OOB;
    t.px = (lane < 20 && idx < 30) ? ((xcd >> 2) * 848 + (idx * 8 + w) * 5 + (lane >> 2)) * 1280 + (3 + (lane & 3)) * 128 : OOB;
    t.dummy = 0;
  }
  {
    const int kb = unit / 24, r24 = unit - kb * 24, ni = r24 / 3, part = r24 - ni * 3;
    const bool has = kb < KB2;
    t.rrs = kb < 20 ? mkrs(P.wo, D_ * D_ * 2) : mkrs(P.wd, F_ * D_ * 2);
    const int kl = (kb + 4) % 6, cs = kb < 16 ? 0 : kb < 20 ? 1 : kl < 4 ? 2 : 3;
    const int base = cs == 0 ? 40 * kb : cs == 1 ? 160 * kb - 2544 : cs == 2 ? (kb - 20) * 32 - 16 * kl : (kb - 20) * 32;
    const int c2 = (0x04401404 >> (cs * 8)) & 255, c3 = (0x08042808 >> (cs * 8)) & 255, c4 = (0x10085014 >> (cs * 8)) & 255;
    const int row = base + ((lane >> 2) & 3) + c2 * ((lane >> 4) & 1) + c3 * (lane >> 5);
    t.rv[0] = has ? row * 1280 + ni * 160 + part * 64 + (lane & 3) * 16 : OOB;
    t.rv[1] = t.rv[0] + c4 * 1280;
    t.rdst = (u32)(size_t)sm + C::RT + w * 2048;
    t.ors = mkrso(P.w2f, W2F_BYTES, has ? (ni * KB2 + kb) * 5120 + part * 2048 : 0);
    t.rso = has ? lane * 16 : OOB;
    t.rso2 = has && part < 2 ? lane * 16 + 1024 : OOB;
    t.ra = C::RT + w * 2048 + (8 * (lane >> 4) + ((lane >> 2) & 3)) * 64 + (lane & 3) * (part == 2 ? 8 : 16);
  }
  sched();
  if (wq == 3 && lane < 24) {
    u32x4 ones = {0x3f803f80u, 0x3f803f80u, 0x3f803f80u, 0x3f803f80u};
    *(AS3 u32x4*)(sm + C::XR + (lane >> 3) * C::XSLOT + (w + 1) * (C::NX * 1024) - 128 + (lane & 7) * 16) = ones;
  }
#pragma unroll
  for (int i = 0; i < C::NR; ++i) {
    const int r = 16 * i + (lane & 15);
    int tok;
    if (ATT) tok = 2 * min(max(mt * UT_ATT - 7 + r, 0), 1023) + wh;
    else tok = tok_of(min((2 * mt + wh) * HT_MLP + r, S_ - 1));
    t.ro[i] = tok * 8;
  }
#pragma unroll
  for (int i = 0; i < 6; ++i)
#pragma unroll
    for (int c = 0; c < 4; ++c) t.acc[i][c] = f32x4{0.f, 0.f, 0.f, 0.f};
  t.tag = stat_pub<2 * C::NW + C::NX>(sl, t.srs, unit, lane);
  sched();
  wait_lgkm<0>();
  wait_vm<C::vm(-1)>();
  bar();
  sched();
  rdS<ATT, 0, 0>(t, sm); rdH<ATT, 0, 0>(t, sm); rdH<ATT, 0, 1>(t, sm); rdH<ATT, 0, 2>(t, sm); rdH<ATT, 0, 3>(t, sm);
  rdS<ATT, 0, 1>(t, sm); rdS<ATT, 0, 2>(t, sm);
  stage<ATT, 0>(t, sm);
  rec_retry<ATT>(t);
  sched();
}

DEVI void k1_mlp(int w, int mt, int fs, const Ptrs& P, AS3 char* sm) {
  char* a2f = P.a2f;
  using C = Cfg<false>;
  const int lane = threadIdx.x & 63, wm = w >> 2, wn = w & 3;
  const int G = lane >> 4, tq = lane & 15;
  CS<false> t;
  {
    int q = (lane >> 2) & 3, p = lane & 3;
    t.wa[0] = opq((8 * G + q) * 384 + ((wn ^ (q >> 1) ^ ((G & 1) << 1)) << 5) + 8 * p);
    t.wa[1] = opq(t.wa[0] + 49152);
    t.xa[0] = opq(C::XR + (wm * 64 + tq) * 128 + ((G ^ (tq >> 1)) << 4));
    t.xa[1] = opq(t.xa[0] ^ 64);
  }
  k1_main<false>(t, w, mt, fs, P, sm);
  f32x4 rc[4];
#pragma unroll
  for (int i = 0; i < 4; ++i) {
    const float den = hi16(t.rec[i][0]), rs = __builtin_amdgcn_rcpf(den);
    rc[i] = f32x4{-lo16(t.rec[i][0]), -2.30220820f * rs, -0.10294324f * rs * rs * rs, den * den};
  }
  u32 vo[4];
#pragma unroll
  for (int i = 0; i < 4; ++i) {
    const int r = i * 16 + tq, sg = (2 * mt + wm) * HT_MLP + r;
    vo[i] = (r < HT_MLP && sg < S_) ? a2f_off(sg) : OOB;
  }
  const rsrc_t ors = mkrso(a2f, A2F_BYTES, (20 + fs * 6 + wn) * 4096);
  const rsrc_t ors2 = mkrso(a2f, A2F_BYTES, (24 + fs * 6 + (wn >> 1)) * 4096 + (wn & 1) * 512);
  u32 hp[3][4][2];
#pragma unroll
  for (int b = 0; b < 3; ++b) {
    f32x4 cg, cu;
#pragma unroll
    for (int r = 0; r < 4; ++r) { cg[r] = dppf<0x15F>(t.acc[2 * b][3][r]); cu[r] = dppf<0x15F>(t.acc[2 * b + 1][3][r]); }
#pragma unroll
    for (int i = 0; i < 4; ++i) {
      const f32x2 nm = bc2(rc[i][0]), ca = bc2(rc[i][1]), cb = bc2(rc[i][2]), cd = bc2(rc[i][3]);
#pragma unroll
      for (int e = 0; e < 2; ++e) {
        f32x2 g = fma2(e ? half2<1>(cg) : half2<0>(cg), nm, e ? half2<1>(t.acc[2 * b][i]) : half2<0>(t.acc[2 * b][i]));
        f32x2 u = fma2(e ? half2<1>(cu) : half2<0>(cu), nm, e ? half2<1>(t.acc[2 * b + 1][i]) : half2<0>(t.acc[2 * b + 1][i]));
        f32x2 z = g * fma2(g * g, cb, ca);
        f32x2 ex = {__builtin_amdgcn_exp2f(z[0]), __builtin_amdgcn_exp2f(z[1])};
        f32x2 d = fma2(ex, cd, cd);
        f32x2 rd = {__builtin_amdgcn_rcpf(d[0]), __builtin_amdgcn_rcpf(d[1])};
        hp[b][i][e] = pk2v(g * u * rd);
      }
      if (b == 1) st128(u32x4{hp[0][i][0], hp[0][i][1], hp[1][i][0], hp[1][i][1]}, ors, vo[i] + G * 256);
      if (b == 2 && (i & 1))
        st128(pair_rows(hp[2][i - 1][0], hp[2][i - 1][1], hp[2][i][0], hp[2][i][1]), ors2, ((G & 1) ? vo[i] : vo[i - 1]) + (G >> 1) * 256);
    }
  }
}

DEVI void k1_att(int w, int mt, int hg, const Ptrs& P, AS3 char* sm) {
  char* a2f = P.a2f;
  using C = Cfg<true>;
  const int lane = threadIdx.x & 63, hl = w & 3, par = w >> 2;
  const int G = lane >> 4, tq = lane & 15, q = (lane >> 2) & 3, p = lane & 3;
  CS<true> t;
  {
    int rho = q + 4 * (G & 1);
    int rowb = (8 * G + q) * 512;
    int hb = hl * 40;
    int o0 = 176 + 8 * p, o1 = 336 + 8 * p;
    int o2 = p == 0 ? 176 + 32 : p == 1 ? 0 : p == 2 ? 8 : 24;
    int o3 = p == 0 ? 336 + 32 : p == 1 ? 32 : p == 2 ? 16 : 8;
    t.wa[0] = opq(rowb + ((o0 + hb + 32 * rho) & 511));
    t.wa[1] = opq(rowb + ((o1 + hb + 32 * rho) & 511));
    t.wa[2] = opq(rowb + ((o2 + hb + 32 * rho) & 511));
    t.wa[3] = opq(rowb + ((o3 + hb + 32 * rho) & 511));
    t.xa[0] = opq(C::XR + (par * 96 + tq) * 128 + ((G ^ (tq >> 1)) << 4));
    t.xa[1] = opq(t.xa[0] ^ 64);
  }
  k1_main<true>(t, w, mt, hg, P, sm);
  f32x2 ms[6];
#pragma unroll
  for (int T = 0; T < 6; ++T) ms[T] = f32x2{-lo16(t.rec[T][0]), __builtin_amdgcn_rcpf(hi16(t.rec[T][0]))};
  f32x4 cs[4];
#pragma unroll
  for (int c = 0; c < 4; ++c)
#pragma unroll
    for (int r = 0; r < 4; ++r) cs[c][r] = dppf<0x15F>(t.acc[5][c][r]);
  AS3 char* vs = sm + w * 6144;
  const u32 g0m = G == 0 ? ~0u : 0u;
  u32x4 qf[6], kf[6];
#pragma unroll
  for (int T = 0; T < 6; ++T) {
    const f32x2 nm = bc2(ms[T][0]), rs = bc2(ms[T][1]), r2 = bc2(G == 0 ? 1.f : ms[T][1]);
    u32 c00 = pk2v(fma2(half2<0>(cs[0]), nm, half2<0>(t.acc[T][0]))), c01 = pk2v(fma2(half2<1>(cs[0]), nm, half2<1>(t.acc[T][0])));
    u32 c10 = pk2v(fma2(half2<0>(cs[1]), nm, half2<0>(t.acc[T][1])) * rs), c11 = pk2v(fma2(half2<1>(cs[1]), nm, half2<1>(t.acc[T][1])) * rs);
    u32 c20 = pk2v(fma2(half2<0>(cs[2]), nm, half2<0>(t.acc[T][2])) * r2), c21 = pk2v(fma2(half2<1>(cs[2]), nm, half2<1>(t.acc[T][2])) * r2);
    u32 c30 = pk2v(fma2(half2<0>(cs[3]), nm, half2<0>(t.acc[T][3])) * rs), c31 = pk2v(fma2(half2<1>(cs[3]), nm, half2<1>(t.acc[T][3])) * rs);
    qf[T] = u32x4{c00, c01, c20 & g0m, c21 & g0m};
    kf[T] = u32x4{c10, c11, c30 & g0m, c31 & g0m};
    *(AS3 u32x4*)(vs + (16 * T + tq) * 64 + ((G ^ ((tq >> 1) & 3)) << 4)) = u32x4{c20, c21, c30, c31};
  }
  int va0, va1;
  {
    int row = 4 * G + q;
    int g0 = p == 0 ? 1 : p == 3 ? 3 : 2, h0 = p == 2 ? 8 : 0;
    va0 = row * 64 + ((g0 ^ ((row >> 1) & 3)) << 4) + h0;
    va1 = row * 64 + ((1 ^ ((row >> 1) & 3)) << 4) + 8;
  }
  const float NEG = -__builtin_inff();
  const f32x4 z4 = {0.f, 0.f, 0.f, 0.f};
  bool oka[4], ok0[4];
  f32x4 bb;
#pragma unroll
  for (int r = 0; r < 4; ++r) {
    const int key = 4 * G + r;
    oka[r] = key <= tq && key >= tq - 7;
    ok0[r] = oka[r] && (mt > 0 || key >= 7);
    bb[r] = key >= tq + 9 ? 0.f : NEG;
  }
  const u32 ma0 = (oka[0] ? 0xffffu : 0u) | (oka[1] ? 0xffff0000u : 0u), ma1 = (oka[2] ? 0xffffu : 0u) | (oka[3] ? 0xffff0000u : 0u);
  const u32x4 ones = {0x3f803f80u, 0x3f803f80u, 0x3f803f80u, 0x3f803f80u};
  const float qs = 0.22360679775f * 1.44269504089f;
  const int H = hg * 4 + hl, u0 = mt * UT_ATT - 7;
  const rsrc_t ors = mkrso(a2f, A2F_BYTES, (H >> 1) * 4096 + (H & 1) * 512);
  const rsrc_t ors2 = mkrso(a2f, A2F_BYTES, (16 + (H >> 3)) * 4096 + ((H >> 1) & 3) * 256 + (H & 1) * 8);
  u32x4 pf[6];
#pragma unroll
  for (int T = 0; T < 6; ++T) {
    f32x4 sa = mfmau(kf[T], qf[T], z4), s;
    if (T > 0) {
      f32x4 sb = mfmau(kf[T > 0 ? T - 1 : 0], qf[T], bb);
#pragma unroll
      for (int r = 0; r < 4; ++r) s[r] = oka[r] ? sa[r] : sb[r];
    } else {
#pragma unroll
      for (int r = 0; r < 4; ++r) s[r] = ok0[r] ? sa[r] : NEG;
    }
    const float mx = xg_max(fmaxf(fmaxf(s[0], s[1]), fmaxf(s[2], s[3])));
    const float c = ms[T][1] * qs;
    const f32x2 e0 = fma2(half2<0>(s), bc2(c), bc2(-mx * c)), e1 = fma2(half2<1>(s), bc2(c), bc2(-mx * c));
    const u32 p0 = pk2(__builtin_amdgcn_exp2f(e0[0]), __builtin_amdgcn_exp2f(e0[1]));
    const u32 p1 = pk2(__builtin_amdgcn_exp2f(e1[0]), __builtin_amdgcn_exp2f(e1[1]));
    pf[T] = u32x4{p0 & ma0, p1 & ma1, p0 & ~ma0, p1 & ~ma1};
  }
  s16x4 vp0 = {0, 0, 0, 0}, vp1 = {0, 0, 0, 0};
  u32 oa[2][2], ob[2][2];
#pragma unroll
  for (int T = 0; T < 6; ++T) {
    s16x4 vc0 = ldtr(vs + va0 + T * 1024), vc1 = ldtr(vs + va1 + T * 1024);
    const bf16x8 pb = __builtin_bit_cast(bf16x8, pf[T]);
    f32x4 sm4 = mfma(__builtin_bit_cast(bf16x8, ones), pb, z4);
    f32x4 o0 = mfma(cat(vc0, vp0), pb, z4);
    f32x4 o1 = mfma(cat(vc1, vp1), pb, z4);
    vp0 = vc0; vp1 = vc1;
    const f32x2 inv = bc2(__builtin_amdgcn_rcpf(sm4[0]));
    oa[T & 1][0] = pk2v(half2<0>(o0) * inv); oa[T & 1][1] = pk2v(half2<1>(o0) * inv);
    ob[T & 1][0] = pk2v(half2<0>(o1) * inv); ob[T & 1][1] = pk2v(half2<1>(o1) * inv);
    if (T & 1) {
      const int i = 16 * (T - 1 + (G & 1)) + tq, u = u0 + i;
      const u32 vo = (i >= 7 && i < 7 + UT_ATT && u < 1024) ? a2f_off((u >> 4) * 32 + par * 16 + (u & 15)) : OOB;
      st128(pair_rows(oa[0][0], oa[0][1], oa[1][0], oa[1][1]), ors, vo + (G >> 1) * 256);
      st64((G & 1) ? u32x2{ob[1][0], ob[1][1]} : u32x2{ob[0][0], ob[0][1]}, ors2, G < 2 ? vo : OOB);
    }
  }
}

__global__ __launch_bounds__(512) void k1(const char* __restrict__ x, const char* __restrict__ wqkv,
                                          const char* __restrict__ wo, const char* __restrict__ wg,
                                          const char* __restrict__ wu, const char* __restrict__ wd,
                                          char* __restrict__ a2f, char* __restrict__ w2f, char* __restrict__ st) {
  __shared__ __attribute__((aligned(1024))) char smem[LDS_K1];
  AS3 char* sm = (AS3 char*)smem;
  const int b = blockIdx.x, xcd = b & 7, idx = b >> 3;
  const int xm = xcd >> 2, xn = xcd & 3;
  const int w = __builtin_amdgcn_readfirstlane(threadIdx.x >> 6);
  if (idx < 12) {
    const int mt = xm * 6 + (idx >> 1), hg = xn * 2 + (idx & 1);
    const Ptrs P = {x, wqkv, wqkv, wo, wd, a2f, w2f, st};
    k1_att(w, mt, hg, P, sm);
    return;
  }
  const int j = idx - 12;
  int mt, fs;
  if (xm == 0) {
    const int q = xn * 20 + j;
    mt = q % 9; fs = q / 9;
  } else {
    if (j >= (xn == 3 ? 19 : 18)) {
      StatLd sl;
      stat_load(sl, mkrs(x, S_ * D_ * 2), mkrs(st, S_ * 8 + 4), b * 8 + w, threadIdx.x & 63);
      stat_pub<0>(sl, mkrs(st, S_ * 8 + 4), b * 8 + w, threadIdx.x & 63);
      return;
    }
    const int e = xn * 18 + j;
    mt = e < 72 ? 9 + (e & 7) : 8; fs = e < 72 ? e >> 3 : 8;
  }
  const Ptrs P = {x, wg, wu, wo, wd, a2f, w2f, st};
  k1_mlp(w, mt, fs, P, sm);
}

constexpr int K2P = 6;
struct K2State {
  f32x4 acc[4][5];
  bf16x8 a[K2P][4], wv[K2P][5];
  const char* pa; const char* pw;
};
template <int I> DEVI void k2_load(K2State& t) {
#pragma unroll
  for (int i = 0; i < 4; ++i) t.a[I % K2P][i] = *(const bf16x8*)(t.pa + (size_t)I * 16384 + i * 1024);
#pragma unroll
  for (int i = 0; i < 5; ++i) t.wv[I % K2P][i] = *(const bf16x8*)(t.pw + (size_t)I * 20480 + i * 1024);
}
template <int I> DEVI void k2_mfma(K2State& t) {
#pragma unroll
  for (int n = 0; n < 5; ++n)
#pragma unroll
    for (int i = 0; i < 4; ++i)
      t.acc[i][n] = __builtin_amdgcn_mfma_f32_16x16x32_bf16(t.wv[I % K2P][n], t.a[I % K2P][i], t.acc[i][n], 0, 0, 0);
}
template <int I> DEVI void k2_step(K2State& t, bool extra) {
  k2_mfma<I>(t);
  sched();
  if constexpr (I + K2P < 18) k2_load<I + K2P>(t);
  if constexpr (I + K2P == 18) { if (extra) k2_load<18>(t); }
  sched();
  if constexpr (I < 17) k2_step<I + 1>(t, extra);
}
template <int I> DEVI void k2_pro(K2State& t) {
  k2_load<I>(t);
  if constexpr (I + 1 < K2P) k2_pro<I + 1>(t);
}

__global__ __launch_bounds__(256, 1) void k2(const char* __restrict__ a2f, const char* __restrict__ w2f,
                                             const char* __restrict__ x, char* __restrict__ out, u32* __restrict__ ep) {
  __shared__ __attribute__((aligned(1024))) char smem[4 * 4 * 5 * 1024];
  AS3 char* sm = (AS3 char*)smem;
  const int b = blockIdx.x, xcd = b & 7, idx = b >> 3;
  const int mi = (xcd >> 1) * 8 + (idx >> 2), ni = (xcd & 1) * 4 + (idx & 3);
  const int lane = threadIdx.x & 63, w = __builtin_amdgcn_readfirstlane(threadIdx.x >> 6);
  K2State t;
  t.pa = a2f + (size_t)(mi * KB2 + w) * 4096 + lane * 16;
  t.pw = w2f + (size_t)(ni * KB2 + w) * 5120 + lane * 16;
  k2_pro<0>(t);
  const int tok = tok_of(mi * 64 + w * 16 + (lane & 15)), G = lane >> 4;
  const size_t ro = (size_t)tok * 1280 + ni * 160;
  u32x4 xr0 = *(const u32x4*)(x + ro + G * 16), xr1 = *(const u32x4*)(x + ro + 64 + G * 16);
  u32x2 xr2 = *(const u32x2*)(x + ro + 128 + G * 8);
#pragma unroll
  for (int i = 0; i < 4; ++i)
#pragma unroll
    for (int n = 0; n < 5; ++n) t.acc[i][n] = f32x4{0.f, 0.f, 0.f, 0.f};
  sched();
  const bool extra = w < 2;
  k2_step<0>(t, extra);
  if (extra) k2_mfma<18>(t);
#pragma unroll
  for (int i = 0; i < 4; ++i)
#pragma unroll
    for (int n = 0; n < 5; ++n) *(AS3 f32x4*)(sm + ((i * 4 + w) * 5 + n) * 1024 + lane * 16) = t.acc[i][n];
  __syncthreads();
  f32x4 r[5];
#pragma unroll
  for (int n = 0; n < 5; ++n) {
    r[n] = *(AS3 f32x4*)(sm + ((w * 4 + 0) * 5 + n) * 1024 + lane * 16);
#pragma unroll
    for (int s = 1; s < 4; ++s) r[n] += *(AS3 f32x4*)(sm + ((w * 4 + s) * 5 + n) * 1024 + lane * 16);
  }
  u32x4 o0 = {pk2(r[0][0] + lo16(xr0[0]), r[0][1] + hi16(xr0[0])), pk2(r[0][2] + lo16(xr0[1]), r[0][3] + hi16(xr0[1])),
              pk2(r[1][0] + lo16(xr0[2]), r[1][1] + hi16(xr0[2])), pk2(r[1][2] + lo16(xr0[3]), r[1][3] + hi16(xr0[3]))};
  u32x4 o1 = {pk2(r[2][0] + lo16(xr1[0]), r[2][1] + hi16(xr1[0])), pk2(r[2][2] + lo16(xr1[1]), r[2][3] + hi16(xr1[1])),
              pk2(r[3][0] + lo16(xr1[2]), r[3][1] + hi16(xr1[2])), pk2(r[3][2] + lo16(xr1[3]), r[3][3] + hi16(xr1[3]))};
  u32x2 o2 = {pk2(r[4][0] + lo16(xr2[0]), r[4][1] + hi16(xr2[0])), pk2(r[4][2] + lo16(xr2[1]), r[4][3] + hi16(xr2[1]))};
  *(u32x4*)(out + ro + G * 16) = o0;
  *(u32x4*)(out + ro + 64 + G * 16) = o1;
  *(u32x2*)(out + ro + 128 + G * 8) = o2;
  if (b == 0 && threadIdx.x == 0) *ep += TAGC;
}

extern "C" void kernel_launch(void* const* d_in, const int* in_sizes, int n_in, void* d_out, int out_size,
                              void* d_ws, size_t ws_size, hipStream_t stream) {
  const char* x = (const char*)d_in[0];
  char* a2f = (char*)d_ws;
  char* w2f = a2f + A2F_BYTES;
  char* st = w2f + W2F_BYTES;
  k1<<<256, 512, 0, stream>>>(x, (const char*)d_in[1], (const char*)d_in[2], (const char*)d_in[4],
                              (const char*)d_in[5], (const char*)d_in[6], a2f, w2f, st);
  k2<<<256, 256, 0, stream>>>(a2f, w2f, x, (char*)d_out, (u32*)(st + S_ * 8));
}
```

```cpp
#include <hip/hip_runtime.h>
#include <stdint.h>

typedef unsigned short u16;
typedef unsigned int u32;
typedef __attribute__((ext_vector_type(8))) __bf16 bf16x8;
typedef __attribute__((ext_vector_type(4))) __bf16 bf16x4;
typedef __attribute__((ext_vector_type(2))) __bf16 bf16x2;
typedef __attribute__((ext_vector_type(4))) short s16x4;
typedef __attribute__((ext_vector_type(8))) short s16x8;
typedef __attribute__((ext_vector_type(2))) float f32x2;
typedef __attribute__((ext_vector_type(4))) float f32x4;
typedef __attribute__((ext_vector_type(16))) float f32x16;
typedef __attribute__((ext_vector_type(4))) u32 u32x4;
typedef __attribute__((ext_vector_type(2))) u32 u32x2;
#define AS3 __attribute__((address_space(3)))
#define AS1 __attribute__((address_space(1)))
#define DEVI __device__ __forceinline__

constexpr int S_ = 2048, D_ = 640, F_ = 1728;
constexpr int KB2 = 74;
constexpr size_t A2F_BYTES = (size_t)32 * KB2 * 4096;
constexpr int MT_MLP = 121;
constexpr int UT_ATT = 86;

template <int N> DEVI void wait_vm() { asm volatile("s_waitcnt vmcnt(%0)" ::"n"(N) : "memory"); }
template <int N> DEVI void wait_lgkm() { asm volatile("s_waitcnt lgkmcnt(%0)" ::"n"(N) : "memory"); }
DEVI void bar() { __builtin_amdgcn_s_barrier(); }
DEVI void sched() { __builtin_amdgcn_sched_barrier(0); }
DEVI u32 opq(u32 v) { asm volatile("" : "+v"(v)); return v; }

DEVI u32 pk2(float a, float b) {
  f32x2 v = {a, b};
  bf16x2 r = __builtin_convertvector(v, bf16x2);
  return __builtin_bit_cast(u32, r);
}
DEVI float rbf(float a) { return __uint_as_float(pk2(a, 0.f) << 16); }
DEVI float lo16(u32 v) { return __uint_as_float(v << 16); }
DEVI float hi16(u32 v) { return __uint_as_float(v & 0xffff0000u); }

typedef __amdgpu_buffer_rsrc_t rsrc_t;
DEVI rsrc_t mkrs(const char* p, int bytes) { return __builtin_amdgcn_make_buffer_rsrc((void*)p, (short)0, bytes, 0x00020000); }
template <int SOFF, int IMM> DEVI void dma16(rsrc_t rs, u32 voff, u32 lds) {
  u32 tmp;
  asm volatile("s_mov_b32 m0, %1\n\ts_mov_b32 %0, %4\n\tbuffer_load_dwordx4 %2, %3, %0 offen offset:%5 lds"
               : "=&s"(tmp) : "s"(lds), "v"(voff), "s"(rs), "i"(SOFF), "i"(IMM) : "memory");
}
template <int SOFF, int IMM> DEVI void dma16m(rsrc_t rs, u32 voff, u32 lds, uint64_t mask) {
  u32 tmp;
  asm volatile("s_mov_b32 m0, %1\n\ts_mov_b32 %0, %4\n\ts_mov_b64 exec, %6\n\tbuffer_load_dwordx4 %2, %3, %0 offen offset:%5 lds\n\ts_mov_b64 exec, -1"
               : "=&s"(tmp) : "s"(lds), "v"(voff), "s"(rs), "i"(SOFF), "i"(IMM), "s"(mask) : "memory");
}
template <int IMM> DEVI void dma16s(rsrc_t rs, u32 voff, u32 soff, u32 lds) {
  asm volatile("s_mov_b32 m0, %0\n\ts_nop 4\n\tbuffer_load_dwordx4 %1, %2, %3 offen offset:%4 lds"
               :: "s"(lds), "v"(voff), "s"(rs), "s"(soff), "i"(IMM) : "memory");
}
DEVI bf16x8 ld128(AS3 char* p) { return *(AS3 bf16x8*)p; }
DEVI s16x4 ldtr(AS3 char* p) { return __builtin_amdgcn_ds_read_tr16_b64_v4i16((AS3 s16x4*)p); }
DEVI bf16x8 cat(s16x4 a, s16x4 b) {
  s16x8 r = __builtin_shufflevector(a, b, 0, 1, 2, 3, 4, 5, 6, 7);
  return __builtin_bit_cast(bf16x8, r);
}
template <int CTRL> DEVI float dppf(float v) {
  return __int_as_float(__builtin_amdgcn_update_dpp(0, __float_as_int(v), CTRL, 0xf, 0xf, true));
}
DEVI float sum8(float v) {
  v += dppf<0xB1>(v);
  v += dppf<0x4E>(v);
  v += dppf<0x141>(v);
  return v;
}
DEVI float xg_max(float x) {
  auto s = __builtin_amdgcn_permlane16_swap(__float_as_uint(x), __float_as_uint(x), false, false);
  x = fmaxf(__uint_as_float(s[0]), __uint_as_float(s[1]));
  auto t = __builtin_amdgcn_permlane32_swap(__float_as_uint(x), __float_as_uint(x), false, false);
  return fmaxf(__uint_as_float(t[0]), __uint_as_float(t[1]));
}
DEVI float xg_sum(float x) {
  auto s = __builtin_amdgcn_permlane16_swap(__float_as_uint(x), __float_as_uint(x), false, false);
  x = __uint_as_float(s[0]) + __uint_as_float(s[1]);
  auto t = __builtin_amdgcn_permlane32_swap(__float_as_uint(x), __float_as_uint(x), false, false);
  return __uint_as_float(t[0]) + __uint_as_float(t[1]);
}

struct RowStat { float c, s1, s2; };
template <bool FIRST, int I> DEVI void stat_acc(RowStat& st, bf16x8 v8) {
  u32x4 v = __builtin_bit_cast(u32x4, v8);
  if (FIRST && I == 0) { st.c = lo16(v[0]); st.s1 = 0.f; st.s2 = 0.f; }
  float a = lo16(v[I]) - st.c, b = hi16(v[I]) - st.c;
  st.s1 += a; st.s2 = fmaf(a, a, st.s2);
  st.s1 += b; st.s2 = fmaf(b, b, st.s2);
  asm volatile("" : "+v"(st.s1), "+v"(st.s2));
}
DEVI f32x2 stat_fin(const RowStat& st) {
  float d = st.s1 * (1.f / 80.f);
  float mi = st.c + d;
  float m2 = fmaf(-st.s1, d, st.s2);
  float mean = sum8(mi) * 0.125f;
  float e = mi - mean;
  m2 = fmaf(80.f * e, e, m2);
  float var = fmaxf(sum8(m2) * (1.f / 640.f), 0.f);
  float den = rbf(sqrtf(rbf(rbf(var) + 1e-5f)));
  f32x2 r = {rbf(mean), 1.f / den};
  return r;
}

constexpr int M_GR = 0, M_UR = 49152, M_XR = 98304, M_ST = 147456, M_CS = M_ST + 1024;
constexpr int LDS_K1 = 150528;

constexpr int mlp_nd(int s, int a) {
  if (a == 1) return (s + 4 < 20) ? 2 : 0;
  int n = 0;
  if (s >= 1 && s + 3 < 20) n += 1;
  if (s >= 2 && (s - 2) / 2 + 3 < 10) n += 1;
  return n;
}
constexpr int mlp_vm(int s) {
  int n = mlp_nd(s, 0);
  if (s >= 1) n += mlp_nd(s - 1, 0) + mlp_nd(s - 1, 1);
  if (s >= 2) n += mlp_nd(s - 2, 1);
  if (s == 0) n += 2 + 3 + 3 + 2;
  if (s == 1) n += 3 + 2;
  if (s == 2) n += 2;
  return n;
}

struct MlpState {
  f32x16 acc[2][3];
  bf16x8 wf[2][3], xf[2][2];
  RowStat st[2];
  u32 wv[3], xv[2];
  u32 wa[3], xa[4], sa;
  rsrc_t wrs, xrs;
  u32 wdst, xdst;
  uint64_t xmask;
};

template <int SST, int A, int B> DEVI void mlp_rdw(MlpState& t, AS3 char* sm) {
  constexpr int wo = (SST & 3) * 12288 + A * 6144;
  t.wf[A][B] = cat(ldtr(sm + t.wa[B] + wo), ldtr(sm + t.wa[B] + wo + 1536));
}
template <int SST, int A, int I> DEVI void mlp_rdx(MlpState& t, AS3 char* sm) {
  t.xf[A][I] = ld128(sm + t.xa[(SST & 1) * 2 + A] + ((SST >> 1) % 3) * 16384 + I * 4096);
}
template <int WS, int I> DEVI void mlp_dma_w(MlpState& t) {
  dma16<WS * 32 * 3456, I * 1024>(t.wrs, t.wv[I], t.wdst + (WS & 3) * 12288);
}
template <int XS, int I> DEVI void mlp_dma_x(MlpState& t) {
  if constexpr (I == 0) dma16<XS * 128, 0>(t.xrs, t.xv[0], t.xdst + (XS % 3) * 16384);
  else dma16m<XS * 128, 1024>(t.xrs, t.xv[1], t.xdst + (XS % 3) * 16384, t.xmask);
}
template <int A, int I, int B> DEVI void mlp_mm(MlpState& t) {
  sched();
  t.acc[I][B] = __builtin_amdgcn_mfma_f32_32x32x16_bf16(t.wf[A][B], t.xf[A][I], t.acc[I][B], 0, 0, 0);
  sched();
}

template <int SST> DEVI void mlp_stage(MlpState& t, AS3 char* sm) {
  mlp_mm<0, 0, 0>(t);
  mlp_rdx<SST, 1, 0>(t, sm); mlp_rdw<SST, 1, 0>(t, sm);
  mlp_mm<0, 0, 1>(t);
  mlp_rdw<SST, 1, 1>(t, sm); mlp_rdw<SST, 1, 2>(t, sm);
  mlp_mm<0, 0, 2>(t);
  mlp_rdx<SST, 1, 1>(t, sm);
  bf16x8 sv = ld128(sm + t.sa + ((SST >> 1) % 3) * 16384 + (SST & 1) * 1024);
  mlp_mm<0, 1, 0>(t);
  if constexpr (SST >= 1 && SST + 3 < 20) mlp_dma_w<SST + 3, 2>(t);
  mlp_mm<0, 1, 1>(t);
  if constexpr (SST >= 2 && (SST - 2) / 2 + 3 < 10) mlp_dma_x<(SST - 2) / 2 + 3, SST & 1>(t);
  mlp_mm<0, 1, 2>(t);
  if constexpr (SST < 19) {
    wait_lgkm<0>();
    wait_vm<mlp_vm(SST)>();
    bar();
  }
  mlp_mm<1, 0, 0>(t);
  if constexpr (SST < 19) { mlp_rdx<SST + 1, 0, 0>(t, sm); mlp_rdw<SST + 1, 0, 0>(t, sm); }
  mlp_mm<1, 0, 1>(t);
  if constexpr (SST < 19) { mlp_rdw<SST + 1, 0, 1>(t, sm); mlp_rdw<SST + 1, 0, 2>(t, sm); }
  stat_acc<(SST < 2), 0>(t.st[SST & 1], sv);
  mlp_mm<1, 0, 2>(t);
  if constexpr (SST < 19) mlp_rdx<SST + 1, 0, 1>(t, sm);
  stat_acc<(SST < 2), 1>(t.st[SST & 1], sv);
  mlp_mm<1, 1, 0>(t);
  if constexpr (SST + 4 < 20) mlp_dma_w<SST + 4, 0>(t);
  stat_acc<(SST < 2), 2>(t.st[SST & 1], sv);
  mlp_mm<1, 1, 1>(t);
  if constexpr (SST + 4 < 20) mlp_dma_w<SST + 4, 1>(t);
  stat_acc<(SST < 2), 3>(t.st[SST & 1], sv);
  mlp_mm<1, 1, 2>(t);
  if constexpr (SST < 19) mlp_stage<SST + 1>(t, sm);
}

DEVI void k1_mlp(int mt, int fs, const char* x, const char* wg, const char* wu, char* a2f, AS3 char* sm) {
  const int tid = threadIdx.x, lane = tid & 63;
  const int w = __builtin_amdgcn_readfirstlane(tid >> 6);
  const int wm = w >> 2, wn = w & 3;
  const int t0 = mt * MT_MLP, f0 = fs * 192;
  MlpState t;
  const int isUp = wm;
  t.wrs = mkrs((isUp ? wu : wg) + f0 * 2, D_ * F_ * 2 - f0 * 2);
#pragma unroll
  for (int i = 0; i < 3; ++i) {
    int P = (3 * wn + i) * 1024 + lane * 16;
    int row = P / 384, rem = P - row * 384;
    int su = (rem >> 5) ^ ((row >> 1) & 1) ^ (isUp << 1);
    t.wv[i] = row * 3456 + su * 32 + (rem & 16) - i * 1024;
  }
  t.wdst = (u32)(size_t)sm + (isUp ? M_UR : M_GR) + wn * 3072;
  t.xrs = mkrs(x, S_ * D_ * 2);
#pragma unroll
  for (int i = 0; i < 2; ++i) {
    int row = 16 * w + 8 * i + (lane >> 3);
    int tok = min(t0 + row, S_ - 1);
    t.xv[i] = tok * 1280 + (((lane & 7) ^ ((row >> 1) & 7)) << 4) - i * 1024;
  }
  t.xdst = (u32)(size_t)sm + M_XR + w * 2048;
  t.xmask = w == 7 ? 0x00ffffffffffffffull : ~0ull;
  mlp_dma_x<0, 0>(t); mlp_dma_x<0, 1>(t);
  mlp_dma_w<0, 0>(t); mlp_dma_w<0, 1>(t); mlp_dma_w<0, 2>(t);
  mlp_dma_w<1, 0>(t); mlp_dma_w<1, 1>(t); mlp_dma_w<1, 2>(t);
  mlp_dma_x<1, 0>(t); mlp_dma_x<1, 1>(t);
  mlp_dma_w<2, 0>(t); mlp_dma_w<2, 1>(t); mlp_dma_w<2, 2>(t);
  mlp_dma_w<3, 0>(t); mlp_dma_w<3, 1>(t); mlp_dma_w<3, 2>(t);
  mlp_dma_x<2, 0>(t); mlp_dma_x<2, 1>(t);
  sched();
  if (w == 7 && lane < 24) {
    u32x4 ones = {0x3f803f80u, 0x3f803f80u, 0x3f803f80u, 0x3f803f80u};
    *(AS3 u32x4*)(sm + M_XR + (lane >> 3) * 16384 + 127 * 128 + (lane & 7) * 16) = ones;
  }
  {
    int G = lane >> 4, q = (lane >> 2) & 3, p = lane & 3;
    int up = p >> 1;
    int row = 8 * (G >> 1) + q;
#pragma unroll
    for (int b = 0; b < 3; ++b) {
      int su = (wn * 3 + b) ^ ((q >> 1) & 1) ^ (up << 1);
      t.wa[b] = opq((up ? M_UR : M_GR) + row * 384 + su * 32 + (G & 1) * 16 + (p & 1) * 8);
    }
    int xr = wm * 64 + (lane & 31);
#pragma unroll
    for (int c = 0; c < 4; ++c) t.xa[c] = opq(M_XR + xr * 128 + (((c * 2 + (lane >> 5)) ^ ((xr >> 1) & 7)) << 4));
    t.sa = opq(M_XR + w * 2048 + lane * 16);
  }
#pragma unroll
  for (int i = 0; i < 2; ++i)
#pragma unroll
    for (int b = 0; b < 3; ++b)
#pragma unroll
      for (int r = 0; r < 16; ++r) t.acc[i][b][r] = 0.f;
  wait_lgkm<0>();
  wait_vm<13>();
  bar();
  sched();
  mlp_rdx<0, 0, 0>(t, sm); mlp_rdw<0, 0, 0>(t, sm); mlp_rdw<0, 0, 1>(t, sm); mlp_rdw<0, 0, 2>(t, sm); mlp_rdx<0, 0, 1>(t, sm);
  mlp_stage<0>(t, sm);

#pragma unroll
  for (int i = 0; i < 2; ++i) {
    f32x2 r = stat_fin(t.st[i]);
    if ((lane & 7) == 0) *(AS3 f32x2*)(sm + M_ST + (16 * w + 8 * i + (lane >> 3)) * 8) = r;
  }
  const int hh = lane >> 5;
  if (wm == 1 && (lane & 31) == 31) {
#pragma unroll
    for (int b = 0; b < 3; ++b)
#pragma unroll
      for (int g = 0; g < 4; ++g) {
        f32x4 v = {t.acc[1][b][4 * g], t.acc[1][b][4 * g + 1], t.acc[1][b][4 * g + 2], t.acc[1][b][4 * g + 3]};
        *(AS3 f32x4*)(sm + M_CS + ((wn * 3 + b) * 32 + 8 * g + 4 * hh) * 4) = v;
      }
  }
  wait_lgkm<0>();
  bar();
  f32x2 ms[2];
#pragma unroll
  for (int i = 0; i < 2; ++i) ms[i] = *(AS3 f32x2*)(sm + M_ST + (wm * 64 + i * 32 + (lane & 31)) * 8);
#pragma unroll
  for (int b = 0; b < 3; ++b) {
    f32x4 cs[4];
#pragma unroll
    for (int g = 0; g < 4; ++g) cs[g] = *(AS3 f32x4*)(sm + M_CS + ((wn * 3 + b) * 32 + 8 * g + 4 * hh) * 4);
    const int F16 = fs * 12 + wn * 3 + b;
    const int kb = 20 + (F16 >> 1), go = (F16 & 1) * 2 + hh;
#pragma unroll
    for (int i = 0; i < 2; ++i) {
      const float nm = -ms[i][0], rs = ms[i][1];
      float h[8];
#pragma unroll
      for (int e = 0; e < 8; ++e) {
        const int gg = (e >> 2) * 2, r = e & 3;
        float g = fmaf(nm, cs[gg][r], t.acc[i][b][4 * gg + r]) * rs;
        float u = fmaf(nm, cs[gg + 1][r], t.acc[i][b][4 * gg + 4 + r]) * rs;
        float z = g * fmaf(g * g, -0.10294324f, -2.30220820f);
        float sg = __builtin_amdgcn_rcpf(1.f + __builtin_amdgcn_exp2f(z));
        h[e] = g * u * sg;
      }
      const int row = wm * 64 + i * 32 + (lane & 31);
      const int tok = t0 + row;
      if (row < MT_MLP && tok < S_) {
        u32x4 o = {pk2(h[0], h[1]), pk2(h[2], h[3]), pk2(h[4], h[5]), pk2(h[6], h[7])};
        *(u32x4*)(a2f + ((size_t)((tok >> 6) * KB2 + kb) * 4096 + ((tok >> 4) & 3) * 1024 + go * 256 + (tok & 15) * 16)) = o;
      }
    }
  }
}

constexpr int A_WR = 0, A_XR = 65536, A_ST = A_XR + 3 * 24576;
constexpr int att_nd(int s, int a) {
  if (a == 1) return (s + 4 < 20) ? 2 : 0;
  if (s >= 2 && (s - 2) / 2 + 3 < 10) return (s & 1) ? 1 : 2;
  return 0;
}
constexpr int att_vm(int s) {
  int n = att_nd(s, 0);
  if (s >= 1) n += att_nd(s - 1, 0) + att_nd(s - 1, 1);
  if (s >= 2) n += att_nd(s - 2, 1);
  if (s == 0) n += 3 + 2 + 2 + 3;
  if (s == 1) n += 2 + 3;
  if (s == 2) n += 3;
  return n;
}
struct AttState {
  f32x4 acc[6][4];
  bf16x8 wf[2][4], xf[2][3];
  RowStat st[3];
  u32 wv[2], xv[3];
  u32 wa[4], xa[2], sa;
  rsrc_t wrs, xrs;
  u32 wdst, xdst;
  uint64_t xmask;
};
template <int SST, int A, int I> DEVI void att_rdx(AttState& t, AS3 char* sm) {
  t.xf[A][I] = ld128(sm + t.xa[SST & 1] + ((SST >> 1) % 3) * 24576 + A * 6144 + I * 2048);
}
template <int SST, int C> DEVI void att_rdw(AttState& t, AS3 char* sm) {
  constexpr int wo = A_WR + (SST & 3) * 16384;
  t.wf[SST & 1][C] = cat(ldtr(sm + t.wa[C] + wo), ldtr(sm + t.wa[C] + wo + 2048));
}
template <int WS, int I> DEVI void att_dma_w(AttState& t) {
  dma16<WS * 32 * 3840, I * 1024>(t.wrs, t.wv[I], t.wdst + (WS & 3) * 16384);
}
template <int XS, int I> DEVI void att_dma_x(AttState& t) {
  if constexpr (I != 2) dma16<XS * 128, I * 1024>(t.xrs, t.xv[I], t.xdst + (XS % 3) * 24576);
  else dma16m<XS * 128, 2048>(t.xrs, t.xv[2], t.xdst + (XS % 3) * 24576, t.xmask);
}
template <int SST, int A, int I, int C2> DEVI void att_mm(AttState& t) {
  sched();
  t.acc[A * 3 + I][C2 * 2] = __builtin_amdgcn_mfma_f32_16x16x32_bf16(t.wf[SST & 1][C2 * 2], t.xf[A][I], t.acc[A * 3 + I][C2 * 2], 0, 0, 0);
  t.acc[A * 3 + I][C2 * 2 + 1] = __builtin_amdgcn_mfma_f32_16x16x32_bf16(t.wf[SST & 1][C2 * 2 + 1], t.xf[A][I], t.acc[A * 3 + I][C2 * 2 + 1], 0, 0, 0);
  sched();
}
template <int SST> DEVI void att_stage(AttState& t, AS3 char* sm) {
  constexpr int so = ((SST >> 1) % 3) * 24576;
  constexpr bool XD = SST >= 2 && (SST - 2) / 2 + 3 < 10;
  constexpr int XS = (SST - 2) / 2 + 3;
  constexpr bool EV = (SST & 1) == 0;
  att_mm<SST, 0, 0, 0>(t);
  att_rdx<SST, 1, 0>(t, sm); att_rdx<SST, 1, 1>(t, sm);
  att_mm<SST, 0, 0, 1>(t);
  att_rdx<SST, 1, 2>(t, sm);
  bf16x8 sv0 = ld128(sm + t.sa + so + (SST & 1) * 2048), sv1;
  att_mm<SST, 0, 1, 0>(t);
  if constexpr (EV) sv1 = ld128(sm + t.sa + so + 1024);
  att_mm<SST, 0, 1, 1>(t);
  if constexpr (XD) att_dma_x<XS, EV ? 0 : 2>(t);
  att_mm<SST, 0, 2, 0>(t);
  if constexpr (XD && EV) att_dma_x<XS, 1>(t);
  att_mm<SST, 0, 2, 1>(t);
  if constexpr (SST < 19) {
    wait_lgkm<0>();
    wait_vm<att_vm(SST)>();
    bar();
  }
  att_mm<SST, 1, 0, 0>(t);
  if constexpr (SST < 19) { att_rdx<SST + 1, 0, 0>(t, sm); att_rdw<SST + 1, 0>(t, sm); att_rdw<SST + 1, 1>(t, sm); }
  stat_acc<(SST < 2), 0>(t.st[(SST & 1) * 2], sv0);
  att_mm<SST, 1, 0, 1>(t);
  if constexpr (SST < 19) { att_rdw<SST + 1, 2>(t, sm); att_rdw<SST + 1, 3>(t, sm); }
  stat_acc<(SST < 2), 1>(t.st[(SST & 1) * 2], sv0);
  att_mm<SST, 1, 1, 0>(t);
  if constexpr (SST < 19) { att_rdx<SST + 1, 0, 1>(t, sm); att_rdx<SST + 1, 0, 2>(t, sm); }
  stat_acc<(SST < 2), 2>(t.st[(SST & 1) * 2], sv0);
  if constexpr (EV) stat_acc<(SST < 2), 0>(t.st[1], sv1);
  att_mm<SST, 1, 1, 1>(t);
  if constexpr (SST + 4 < 20) att_dma_w<SST + 4, 0>(t);
  stat_acc<(SST < 2), 3>(t.st[(SST & 1) * 2], sv0);
  if constexpr (EV) stat_acc<(SST < 2), 1>(t.st[1], sv1);
  att_mm<SST, 1, 2, 0>(t);
  if constexpr (SST + 4 < 20) att_dma_w<SST + 4, 1>(t);
  if constexpr (EV) stat_acc<(SST < 2), 2>(t.st[1], sv1);
  att_mm<SST, 1, 2, 1>(t);
  if constexpr (EV) stat_acc<(SST < 2), 3>(t.st[1], sv1);
  if constexpr (SST < 19) att_stage<SST + 1>(t, sm);
}

DEVI void k1_att(int mt, int hg, const char* x, const char* wqkv, char* a2f, AS3 char* sm) {
  const int tid = threadIdx.x, lane = tid & 63;
  const int w = __builtin_amdgcn_readfirstlane(tid >> 6);
  const int hl = w & 3, par = w >> 2;
  const int u0 = mt * UT_ATT - 7;
  AttState t;
  t.wrs = mkrs(wqkv + hg * 160, D_ * 3 * D_ * 2 - hg * 160);
#pragma unroll
  for (int i = 0; i < 2; ++i) {
    int row = 4 * w + 2 * i + (lane >> 5);
    int rho = (row & 3) + 4 * ((row >> 3) & 1);
    int c = ((lane & 31) - 2 * rho) & 31;
    int so = c < 10 ? 2560 + c * 16 : (c == 10 || c == 31) ? 0 : c <= 20 ? (c - 11) * 16 : 1280 + (c - 21) * 16;
    t.wv[i] = row * 3840 + so - i * 1024;
  }
  t.wdst = (u32)(size_t)sm + A_WR + w * 2048;
  t.xrs = mkrs(x - 2048, S_ * D_ * 2 + 2048);
#pragma unroll
  for (int i = 0; i < 3; ++i) {
    int row = 24 * w + 8 * i + (lane >> 3);
    int pr = row >= 96 ? 1 : 0;
    int u = min(max(u0 + row - pr * 96, 0), 1023);
    t.xv[i] = (2 * u + pr) * 1280 + (((lane & 7) ^ ((row >> 1) & 7)) << 4) + 2048 - i * 1024;
  }
  t.xdst = (u32)(size_t)sm + A_XR + w * 3072;
  t.xmask = hl == 3 ? 0x00ffffffffffffffull : ~0ull;
  att_dma_x<0, 0>(t); att_dma_x<0, 1>(t); att_dma_x<0, 2>(t);
  att_dma_w<0, 0>(t); att_dma_w<0, 1>(t);
  att_dma_w<1, 0>(t); att_dma_w<1, 1>(t);
  att_dma_x<1, 0>(t); att_dma_x<1, 1>(t); att_dma_x<1, 2>(t);
  att_dma_w<2, 0>(t); att_dma_w<2, 1>(t);
  att_dma_w<3, 0>(t); att_dma_w<3, 1>(t);
  att_dma_x<2, 0>(t); att_dma_x<2, 1>(t); att_dma_x<2, 2>(t);
  sched();
  if (hl == 3 && lane < 24) {
    u32x4 ones = {0x3f803f80u, 0x3f803f80u, 0x3f803f80u, 0x3f803f80u};
    *(AS3 u32x4*)(sm + A_XR + (lane >> 3) * 24576 + (par * 96 + 95) * 128 + (lane & 7) * 16) = ones;
  }
  const int G = lane >> 4, tq = lane & 15;
  {
    int q = (lane >> 2) & 3, p = lane & 3;
    int rho = q + 4 * (G & 1);
    int rowb = (8 * G + q) * 512;
    int hb = hl * 40;
    int o0 = 176 + 8 * p, o1 = 336 + 8 * p;
    int o2 = p == 0 ? 176 + 32 : p == 1 ? 0 : p == 2 ? 8 : 24;
    int o3 = p == 0 ? 336 + 32 : p == 1 ? 32 : p == 2 ? 16 : 8;
    t.wa[0] = opq(rowb + ((o0 + hb + 32 * rho) & 511));
    t.wa[1] = opq(rowb + ((o1 + hb + 32 * rho) & 511));
    t.wa[2] = opq(rowb + ((o2 + hb + 32 * rho) & 511));
    t.wa[3] = opq(rowb + ((o3 + hb + 32 * rho) & 511));
    int xr = par * 96 + tq;
    t.xa[0] = opq(A_XR + xr * 128 + ((G ^ (tq >> 1)) << 4));
    t.xa[1] = opq(t.xa[0] ^ 64);
    t.sa = opq(A_XR + w * 3072 + lane * 16);
  }
#pragma unroll
  for (int i = 0; i < 6; ++i)
#pragma unroll
    for (int c = 0; c < 4; ++c) t.acc[i][c] = f32x4{0.f, 0.f, 0.f, 0.f};
  wait_lgkm<0>();
  wait_vm<12>();
  bar();
  sched();
  att_rdx<0, 0, 0>(t, sm); att_rdw<0, 0>(t, sm); att_rdw<0, 1>(t, sm); att_rdw<0, 2>(t, sm); att_rdw<0, 3>(t, sm);
  att_rdx<0, 0, 1>(t, sm); att_rdx<0, 0, 2>(t, sm);
  att_stage<0>(t, sm);

#pragma unroll
  for (int i = 0; i < 3; ++i) {
    f32x2 r = stat_fin(t.st[i]);
    if ((lane & 7) == 0) *(AS3 f32x2*)(sm + A_ST + (24 * w + 8 * i + (lane >> 3)) * 8) = r;
  }
  f32x4 cs[4];
#pragma unroll
  for (int c = 0; c < 4; ++c)
#pragma unroll
    for (int r = 0; r < 4; ++r) cs[c][r] = dppf<0x15F>(t.acc[5][c][r]);
  wait_lgkm<0>();
  bar();
  AS3 char* vs = sm + A_WR + w * 6144;
  const float qs = 0.22360679775f * 1.44269504089f;
  u32x4 qf[6], kf[6];
#pragma unroll
  for (int T = 0; T < 6; ++T) {
    f32x2 ms = *(AS3 f32x2*)(sm + A_ST + (par * 96 + 16 * T + tq) * 8);
    const float nm = -ms[0], rs = ms[1], rq = rs * qs, r2 = G == 0 ? rq : rs;
    float v[4][4];
#pragma unroll
    for (int c = 0; c < 4; ++c)
#pragma unroll
      for (int r = 0; r < 4; ++r)
        v[c][r] = fmaf(nm, cs[c][r], t.acc[T][c][r]) * (c == 0 ? rq : c == 2 ? r2 : rs);
    u32 c20 = pk2(v[2][0], v[2][1]), c21 = pk2(v[2][2], v[2][3]);
    u32 c30 = pk2(v[3][0], v[3][1]), c31 = pk2(v[3][2], v[3][3]);
    qf[T] = u32x4{pk2(v[0][0], v[0][1]), pk2(v[0][2], v[0][3]), G == 0 ? c20 : 0u, G == 0 ? c21 : 0u};
    kf[T] = u32x4{pk2(v[1][0], v[1][1]), pk2(v[1][2], v[1][3]), G == 0 ? c30 : 0u, G == 0 ? c31 : 0u};
    *(AS3 u32x4*)(vs + (16 * T + tq) * 64 + ((G ^ ((tq >> 1) & 3)) << 4)) = u32x4{c20, c21, c30, c31};
  }
  int va0, va1;
  {
    int q = (lane >> 2) & 3, p = lane & 3;
    int row = 4 * G + q;
    int g0 = p == 0 ? 1 : p == 3 ? 3 : 2, h0 = p == 2 ? 8 : 0;
    va0 = row * 64 + ((g0 ^ ((row >> 1) & 3)) << 4) + h0;
    va1 = row * 64 + ((1 ^ ((row >> 1) & 3)) << 4) + 8;
  }
  const int H = hg * 4 + hl;
  s16x4 vp0 = {0, 0, 0, 0}, vp1 = {0, 0, 0, 0};
#pragma unroll
  for (int T = 0; T < 6; ++T) {
    s16x4 vc0 = ldtr(vs + va0 + T * 1024), vc1 = ldtr(vs + va1 + T * 1024);
    const f32x4 z4 = {0.f, 0.f, 0.f, 0.f};
    f32x4 sa = __builtin_amdgcn_mfma_f32_16x16x32_bf16(__builtin_bit_cast(bf16x8, kf[T]), __builtin_bit_cast(bf16x8, qf[T]), z4, 0, 0, 0);
    f32x4 sb = z4;
    if (T > 0) sb = __builtin_amdgcn_mfma_f32_16x16x32_bf16(__builtin_bit_cast(bf16x8, kf[T > 0 ? T - 1 : 0]), __builtin_bit_cast(bf16x8, qf[T]), z4, 0, 0, 0);
    const float NEG = -__builtin_inff();
    float mx = NEG;
#pragma unroll
    for (int r = 0; r < 4; ++r) {
      int key = 4 * G + r;
      bool oka = key <= tq && key >= tq - 7 && (u0 + 16 * T + key >= 0 || key == tq);
      bool okb = T > 0 && key >= tq + 9 && (u0 + 16 * (T - 1) + key >= 0);
      sa[r] = oka ? sa[r] : NEG;
      sb[r] = okb ? sb[r] : NEG;
      mx = fmaxf(mx, fmaxf(sa[r], sb[r]));
    }
    mx = xg_max(mx);
    float sum = 0.f;
#pragma unroll
    for (int r = 0; r < 4; ++r) {
      sa[r] = __builtin_amdgcn_exp2f(sa[r] - mx);
      sb[r] = __builtin_amdgcn_exp2f(sb[r] - mx);
      sum += sa[r] + sb[r];
    }
    sum = xg_sum(sum);
    const float inv = __builtin_amdgcn_rcpf(sum);
    u32x4 pf = {pk2(sa[0] * inv, sa[1] * inv), pk2(sa[2] * inv, sa[3] * inv), pk2(sb[0] * inv, sb[1] * inv), pk2(sb[2] * inv, sb[3] * inv)};
    f32x4 o0 = __builtin_amdgcn_mfma_f32_16x16x32_bf16(cat(vc0, vp0), __builtin_bit_cast(bf16x8, pf), z4, 0, 0, 0);
    f32x4 o1 = __builtin_amdgcn_mfma_f32_16x16x32_bf16(cat(vc1, vp1), __builtin_bit_cast(bf16x8, pf), z4, 0, 0, 0);
    vp0 = vc0; vp1 = vc1;
    const int i = 16 * T + tq, u = u0 + i;
    if (i >= 7 && i < 7 + UT_ATT && u < 1024) {
      const int tok = 2 * u + par;
      char* base = a2f + (size_t)(tok >> 6) * KB2 * 4096 + ((tok >> 4) & 3) * 1024 + (tok & 15) * 16;
      const int hA = H * 5 + G;
      *(u32x2*)(base + (hA >> 3) * 4096 + ((hA >> 1) & 3) * 256 + (hA & 1) * 8) = u32x2{pk2(o0[0], o0[1]), pk2(o0[2], o0[3])};
      if (G == 0) {
        const int hB = H * 5 + 4;
        *(u32x2*)(base + (hB >> 3) * 4096 + ((hB >> 1) & 3) * 256 + (hB & 1) * 8) = u32x2{pk2(o1[0], o1[1]), pk2(o1[2], o1[3])};
      }
    }
  }
}

DEVI void k1_retile(int id, int nid, const char* wo, const char* wd, char* w2f, AS3 char* sm) {
  const int tid = threadIdx.x, lane = tid & 63;
  const int w = __builtin_amdgcn_readfirstlane(tid >> 6);
  const int G = lane >> 4, q = (lane >> 2) & 3, p = lane & 3;
  const rsrc_t rso = mkrs(wo, D_ * D_ * 2), rsd = mkrs(wd, F_ * D_ * 2);
  const u32 vo = w * 5120 + lane * 16;
  auto issue = [&](int kb, int slot) {
    const rsrc_t rs = kb < 20 ? rso : rsd;
    const u32 so = __builtin_amdgcn_readfirstlane((kb < 20 ? kb : kb - 20) * 40960), ld = __builtin_amdgcn_readfirstlane((u32)(size_t)sm + slot * 40960 + w * 5120);
    dma16s<0>(rs, vo, so, ld); dma16s<1024>(rs, vo, so, ld); dma16s<2048>(rs, vo, so, ld); dma16s<3072>(rs, vo, so, ld);
    dma16s<0>(rs, vo, so + 4096, ld + 4096);
  };
  const int n = (KB2 - id + nid - 1) / nid;
  issue(id, 0);
  issue(min(id + nid, KB2 - 1), 1);
  int slot = 0;
  for (int it = 0; it < n; ++it) {
    const int kb = id + it * nid;
    if (it == 0) wait_vm<5>(); else wait_vm<10>();
    bar();
    int s2 = slot + 2; if (s2 >= 3) s2 -= 3;
    issue(min(kb + 2 * nid, KB2 - 1), s2);
    const int r0 = kb < 20 ? 8 * G + q : (G >> 1) * 16 + (G & 1) * 4 + q;
    const int rd = kb < 20 ? 4 : 8;
    AS3 char* b = sm + slot * 40960 + r0 * 1280 + w * 160;
    u32x4 o[5];
#pragma unroll
    for (int nb = 0; nb < 5; ++nb) {
      int cb = nb < 4 ? (nb >> 1) * 64 + 16 * p + 8 * (nb & 1) : 128 + 8 * p;
      o[nb] = __builtin_bit_cast(u32x4, cat(ldtr(b + cb), ldtr(b + cb + rd * 1280)));
    }
#pragma unroll
    for (int nb = 0; nb < 5; ++nb) *(u32x4*)(w2f + (size_t)((w * KB2 + kb) * 5 + nb) * 1024 + lane * 16) = o[nb];
    slot = slot + 1 >= 3 ? 0 : slot + 1;
  }
  wait_vm<0>();
}

__global__ __launch_bounds__(512, 2) void k1(const char* __restrict__ x, const char* __restrict__ wqkv,
                                             const char* __restrict__ wo, const char* __restrict__ wg,
                                             const char* __restrict__ wu, const char* __restrict__ wd,
                                             char* __restrict__ a2f, char* __restrict__ w2f) {
  __shared__ __attribute__((aligned(1024))) char smem[LDS_K1];
  AS3 char* sm = (AS3 char*)smem;
  const int b = blockIdx.x, xcd = b & 7, idx = b >> 3;
  const int xm = xcd >> 2, xn = xcd & 3;
  if (idx < 12) {
    k1_att(xm * 6 + (idx >> 1), xn * 2 + (idx & 1), x, wqkv, a2f, sm);
    return;
  }
  const int j = idx - 12;
  int mt, fs;
  if (xm == 0) {
    const int q = xn * 20 + j;
    mt = q % 9; fs = q / 9;
  } else {
    const int nt = xn == 3 ? 19 : 18;
    if (j >= nt) {
      k1_retile(xn * 2 + (j - nt), 7, wo, wd, w2f, sm);
      return;
    }
    const int e = xn * 18 + j;
    mt = e < 72 ? 9 + (e & 7) : 8; fs = e < 72 ? e >> 3 : 8;
  }
  k1_mlp(mt, fs, x, wg, wu, a2f, sm);
}

constexpr int K2P = 6;
struct K2State {
  f32x4 acc[4][5];
  bf16x8 a[K2P][4], wv[K2P][5];
  const char* pa; const char* pw;
};
template <int I> DEVI void k2_load(K2State& t) {
#pragma unroll
  for (int i = 0; i < 4; ++i) t.a[I % K2P][i] = *(const bf16x8*)(t.pa + (size_t)I * 16384 + i * 1024);
#pragma unroll
  for (int i = 0; i < 5; ++i) t.wv[I % K2P][i] = *(const bf16x8*)(t.pw + (size_t)I * 20480 + i * 1024);
}
template <int I> DEVI void k2_mfma(K2State& t) {
#pragma unroll
  for (int n = 0; n < 5; ++n)
#pragma unroll
    for (int i = 0; i < 4; ++i)
      t.acc[i][n] = __builtin_amdgcn_mfma_f32_16x16x32_bf16(t.wv[I % K2P][n], t.a[I % K2P][i], t.acc[i][n], 0, 0, 0);
}
template <int I> DEVI void k2_step(K2State& t, bool extra) {
  k2_mfma<I>(t);
  sched();
  if constexpr (I + K2P < 18) k2_load<I + K2P>(t);
  if constexpr (I + K2P == 18) { if (extra) k2_load<18>(t); }
  sched();
  if constexpr (I < 17) k2_step<I + 1>(t, extra);
}
template <int I> DEVI void k2_pro(K2State& t) {
  k2_load<I>(t);
  if constexpr (I + 1 < K2P) k2_pro<I + 1>(t);
}

__global__ __launch_bounds__(256, 1) void k2(const char* __restrict__ a2f, const char* __restrict__ w2f,
                                             const char* __restrict__ x, char* __restrict__ out) {
  __shared__ __attribute__((aligned(1024))) char smem[4 * 4 * 5 * 1024];
  AS3 char* sm = (AS3 char*)smem;
  const int b = blockIdx.x, xcd = b & 7, idx = b >> 3;
  const int mi = (xcd >> 1) * 8 + (idx >> 2), ni = (xcd & 1) * 4 + (idx & 3);
  const int lane = threadIdx.x & 63, w = __builtin_amdgcn_readfirstlane(threadIdx.x >> 6);
  K2State t;
  t.pa = a2f + (size_t)(mi * KB2 + w) * 4096 + lane * 16;
  t.pw = w2f + (size_t)(ni * KB2 + w) * 5120 + lane * 16;
  k2_pro<0>(t);
  const int tok = mi * 64 + w * 16 + (lane & 15), G = lane >> 4;
  const size_t ro = (size_t)tok * 1280 + ni * 160;
  u32x4 xr0 = *(const u32x4*)(x + ro + G * 16), xr1 = *(const u32x4*)(x + ro + 64 + G * 16);
  u32x2 xr2 = *(const u32x2*)(x + ro + 128 + G * 8);
#pragma unroll
  for (int i = 0; i < 4; ++i)
#pragma unroll
    for (int n = 0; n < 5; ++n) t.acc[i][n] = f32x4{0.f, 0.f, 0.f, 0.f};
  sched();
  const bool extra = w < 2;
  k2_step<0>(t, extra);
  if (extra) k2_mfma<18>(t);
#pragma unroll
  for (int i = 0; i < 4; ++i)
#pragma unroll
    for (int n = 0; n < 5; ++n) *(AS3 f32x4*)(sm + ((i * 4 + w) * 5 + n) * 1024 + lane * 16) = t.acc[i][n];
  __syncthreads();
  f32x4 r[5];
#pragma unroll
  for (int n = 0; n < 5; ++n) {
    r[n] = *(AS3 f32x4*)(sm + ((w * 4 + 0) * 5 + n) * 1024 + lane * 16);
#pragma unroll
    for (int s = 1; s < 4; ++s) r[n] += *(AS3 f32x4*)(sm + ((w * 4 + s) * 5 + n) * 1024 + lane * 16);
  }
  u32x4 o0 = {pk2(r[0][0] + lo16(xr0[0]), r[0][1] + hi16(xr0[0])), pk2(r[0][2] + lo16(xr0[1]), r[0][3] + hi16(xr0[1])),
              pk2(r[1][0] + lo16(xr0[2]), r[1][1] + hi16(xr0[2])), pk2(r[1][2] + lo16(xr0[3]), r[1][3] + hi16(xr0[3]))};
  u32x4 o1 = {pk2(r[2][0] + lo16(xr1[0]), r[2][1] + hi16(xr1[0])), pk2(r[2][2] + lo16(xr1[1]), r[2][3] + hi16(xr1[1])),
              pk2(r[3][0] + lo16(xr1[2]), r[3][1] + hi16(xr1[2])), pk2(r[3][2] + lo16(xr1[3]), r[3][3] + hi16(xr1[3]))};
  u32x2 o2 = {pk2(r[4][0] + lo16(xr2[0]), r[4][1] + hi16(xr2[0])), pk2(r[4][2] + lo16(xr2[1]), r[4][3] + hi16(xr2[1]))};
  *(u32x4*)(out + ro + G * 16) = o0;
  *(u32x4*)(out + ro + 64 + G * 16) = o1;
  *(u32x2*)(out + ro + 128 + G * 8) = o2;
}

extern "C" void kernel_launch(void* const* d_in, const int* in_sizes, int n_in, void* d_out, int out_size,
                              void* d_ws, size_t ws_size, hipStream_t stream) {
  const char* x = (const char*)d_in[0];
  char* a2f = (char*)d_ws;
  char* w2f = a2f + A2F_BYTES;
  k1<<<256, 512, 0, stream>>>(x, (const char*)d_in[1], (const char*)d_in[2], (const char*)d_in[4],
                              (const char*)d_in[5], (const char*)d_in[6], a2f, w2f);
  k2<<<256, 256, 0, stream>>>(a2f, w2f, x, (char*)d_out);
}
```

```cpp
#define MEAS 0
#include <hip/hip_runtime.h>
#include <stdint.h>

typedef unsigned short u16;
typedef unsigned int u32;
typedef __attribute__((ext_vector_type(8))) __bf16 bf16x8;
typedef __attribute__((ext_vector_type(2))) __bf16 bf16x2;
typedef __attribute__((ext_vector_type(4))) short s16x4;
typedef __attribute__((ext_vector_type(8))) short s16x8;
typedef __attribute__((ext_vector_type(2))) float f32x2;
typedef __attribute__((ext_vector_type(4))) float f32x4;
typedef __attribute__((ext_vector_type(4))) u32 u32x4;
typedef __attribute__((ext_vector_type(2))) u32 u32x2;
#define AS3 __attribute__((address_space(3)))
#define DEVI __device__ __forceinline__

constexpr int S_ = 2048, D_ = 640, F_ = 1728;
constexpr int KB2 = 74;
constexpr int A2F_BYTES = 32 * KB2 * 4096;
constexpr int W2F_BYTES = 8 * KB2 * 5120;
constexpr int HT_MLP = 61;
constexpr int UT_ATT = 86;
#define CPRIO 0
#define LPRIO 3
#define ST_AUX 16

template <int N> DEVI void wait_vm() { asm volatile("s_waitcnt vmcnt(%0)" ::"n"(N) : "memory"); }
template <int N> DEVI void wait_lgkm() { asm volatile("s_waitcnt lgkmcnt(%0)" ::"n"(N) : "memory"); }
DEVI void bar() { __builtin_amdgcn_s_barrier(); }
DEVI void sched() { __builtin_amdgcn_sched_barrier(0); }
DEVI u32 opq(u32 v) { asm volatile("" : "+v"(v)); return v; }

DEVI u32 pk2(float a, float b) {
  f32x2 v = {a, b};
  bf16x2 r = __builtin_convertvector(v, bf16x2);
  return __builtin_bit_cast(u32, r);
}
DEVI u32 pk2v(f32x2 v) { return __builtin_bit_cast(u32, __builtin_convertvector(v, bf16x2)); }
DEVI float rbf(float a) { return __uint_as_float(pk2(a, 0.f) << 16); }
DEVI float lo16(u32 v) { return __uint_as_float(v << 16); }
DEVI float hi16(u32 v) { return __uint_as_float(v & 0xffff0000u); }
DEVI f32x2 bc2(float a) { return f32x2{a, a}; }
DEVI f32x2 fma2(f32x2 a, f32x2 b, f32x2 c) { return __builtin_elementwise_fma(a, b, c); }
template <int I> DEVI f32x2 half2(f32x4 v) { return __builtin_shufflevector(v, v, 2 * I, 2 * I + 1); }

typedef __amdgpu_buffer_rsrc_t rsrc_t;
DEVI rsrc_t mkrs(const char* p, int bytes) { return __builtin_amdgcn_make_buffer_rsrc((void*)p, (short)0, bytes, 0x00020000); }
template <int LOFF> DEVI void dma16(rsrc_t rs, u32 voff, u32 soff, u32 lds) {
  asm volatile("s_add_u32 m0, %0, %4\n\ts_nop 0\n\tbuffer_load_dwordx4 %1, %2, %3 offen lds"
               :: "s"(lds), "v"(voff), "s"(rs), "s"(soff), "i"(LOFF) : "memory", "scc");
}
template <int LOFF> DEVI void dma16m(rsrc_t rs, u32 voff, u32 soff, u32 lds, uint64_t mask) {
  asm volatile("s_add_u32 m0, %0, %4\n\ts_mov_b64 exec, %5\n\tbuffer_load_dwordx4 %1, %2, %3 offen lds\n\ts_mov_b64 exec, -1"
               :: "s"(lds), "v"(voff), "s"(rs), "s"(soff), "i"(LOFF), "s"(mask) : "memory", "scc");
}
DEVI void st128(u32x4 v, rsrc_t rs, u32 voff) { __builtin_amdgcn_raw_buffer_store_b128(v, rs, voff, 0, ST_AUX); }
DEVI void st64(u32x2 v, rsrc_t rs, u32 voff) { __builtin_amdgcn_raw_buffer_store_b64(v, rs, voff, 0, ST_AUX); }
DEVI rsrc_t mkrso(const char* p, int bytes, int off) { return mkrs(p + off, bytes - off); }
DEVI bf16x8 ld128(AS3 char* p) { return *(AS3 bf16x8*)p; }
DEVI s16x4 ldtr(AS3 char* p) { return __builtin_amdgcn_ds_read_tr16_b64_v4i16((AS3 s16x4*)p); }
DEVI bf16x8 cat(s16x4 a, s16x4 b) {
  s16x8 r = __builtin_shufflevector(a, b, 0, 1, 2, 3, 4, 5, 6, 7);
  return __builtin_bit_cast(bf16x8, r);
}
DEVI f32x4 mfma(bf16x8 a, bf16x8 b, f32x4 c) { return __builtin_amdgcn_mfma_f32_16x16x32_bf16(a, b, c, 0, 0, 0); }
DEVI f32x4 mfmau(u32x4 a, u32x4 b, f32x4 c) { return mfma(__builtin_bit_cast(bf16x8, a), __builtin_bit_cast(bf16x8, b), c); }
template <int CTRL> DEVI float dppf(float v) {
  return __int_as_float(__builtin_amdgcn_update_dpp(0, __float_as_int(v), CTRL, 0xf, 0xf, true));
}
DEVI float sum8(float v) {
  v += dppf<0xB1>(v);
  v += dppf<0x4E>(v);
  v += dppf<0x141>(v);
  return v;
}
DEVI float xg_max(float x) {
  auto s = __builtin_amdgcn_permlane16_swap(__float_as_uint(x), __float_as_uint(x), false, false);
  x = fmaxf(__uint_as_float(s[0]), __uint_as_float(s[1]));
  auto t = __builtin_amdgcn_permlane32_swap(__float_as_uint(x), __float_as_uint(x), false, false);
  return fmaxf(__uint_as_float(t[0]), __uint_as_float(t[1]));
}
DEVI u32x4 pair_rows(u32 a0, u32 a1, u32 b0, u32 b1) {
  auto s = __builtin_amdgcn_permlane16_swap(a0, b0, false, false);
  auto t = __builtin_amdgcn_permlane16_swap(a1, b1, false, false);
  return u32x4{s[0], t[0], s[1], t[1]};
}
DEVI int tok_of(int sg) { return (sg & ~31) | ((sg & 15) << 1) | ((sg >> 4) & 1); }
DEVI u32 a2f_off(int sg) { return (sg >> 6) * (KB2 * 4096) + ((sg >> 4) & 3) * 1024 + (sg & 15) * 16; }
constexpr u32 OOB = 0x80000000u;

#define CAP 5
#define PLO 9
#define PHI 22
#define RINT 8
#define PFW0 0
#define PFW1 3
#define PFW2 7
#define PFX0 0
#define PFX1 4
enum { T_X, T_W, T_R, T_S, T_REC, T_PW, T_PX };
struct Sched { int n, ty[128], idx[128], sub[128], first[23], vm[21]; };
constexpr Sched mk_sched(int NW, int NX, int NR) {
  Sched s = {};
  int al[128] = {}, du[128] = {}, ty[128] = {}, ix[128] = {}, sb[128] = {}, n = 0;
  for (int j = 0; j < 10; ++j) {
    for (int p = 0; p < NX; ++p) { ty[n] = T_X; ix[n] = j; sb[n] = p; al[n] = j < 3 ? -1 : 2 * j - 4; du[n] = 2 * j - 1; ++n; }
    for (int i = 2 * j; i < 2 * j + 2; ++i)
      for (int p = 0; p < NW; ++p) { ty[n] = T_W; ix[n] = i; sb[n] = p; al[n] = i < 4 ? -1 : i - 3; du[n] = i - 1; ++n; }
  }
  int due[128] = {}, q = 0;
  for (int m = -1; m <= 20; ++m) {
    s.first[m + 1] = s.n;
    int c = 0;
    while (q < n && c < (m < 0 ? NX + 2 * NW : CAP) && al[q] <= m) {
      s.ty[s.n] = ty[q]; s.idx[s.n] = ix[q]; s.sub[s.n] = sb[q]; due[s.n] = du[q]; ++s.n; ++q; ++c;
    }
    if (m == -1) { s.ty[s.n] = T_S; due[s.n] = 99; ++s.n; }
    if (m == PFW0 || m == PFW1 || m == PFW2) { s.ty[s.n] = T_PW; s.sub[s.n] = m == PFW0 ? 0 : m == PFW1 ? 192 : 320; due[s.n] = 99; ++s.n; }
    if (m == PFX0 || m == PFX1) { s.ty[s.n] = T_PX; s.sub[s.n] = m == PFX0 ? 0 : 384; due[s.n] = 99; ++s.n; }
    if (m == RINT) for (int p = 0; p < 2; ++p) { s.ty[s.n] = T_R; s.sub[s.n] = p; due[s.n] = 18; ++s.n; }
    if (m == 17) for (int p = 0; p < NR; ++p) { s.ty[s.n] = T_REC; s.sub[s.n] = p; due[s.n] = 99; ++s.n; }
    if (m <= 19) {
      int need = 0;
      for (int k = 0; k < s.n; ++k) if (due[k] <= m) need = k + 1;
      s.vm[m + 1] = s.n - need;
    }
  }
  s.first[22] = s.n;
  return s;
}
template <bool ATT> struct Cfg {
  static constexpr int NW = ATT ? 2 : 3, NX = ATT ? 3 : 2;
  static constexpr int NR = ATT ? 6 : 4;
  static constexpr int WSLOT = NW * 8192, XSLOT = NX * 8192;
  static constexpr int XR = 4 * WSLOT, RT = XR + 3 * XSLOT;
  static constexpr int WSTEP = 32 * (ATT ? 3 * D_ * 2 : F_ * 2);
  static constexpr Sched SC = mk_sched(NW, NX, NR);
  static constexpr int vm(int m) { return SC.vm[m + 1]; }
  static constexpr int at(int m, int r) {
    const int f = SC.first[m + 1], c = SC.first[m + 2] - f;
    const int lo = m == 0 ? 10 : c > 4 ? 1 : PLO;
    for (int k = 0; k < c; ++k) if (lo + (c > 1 ? k * (PHI - lo) / (c - 1) : 0) == r) return f + k;
    return -1;
  }
};
constexpr int LDS_K1 = 163840;
constexpr u32 TAGC = 0x9E3779B9u;

struct StatLd { u32x4 a; u32 b, e; };
DEVI void stat_load(StatLd& s, rsrc_t xrs, rsrc_t srs, int tok, int lane) {
  asm volatile("buffer_load_dwordx4 %0, %3, %5, %6 offen\n\tbuffer_load_dword %1, %4, %5, %6 offen offset:1024\n\t"
               "buffer_load_dword %2, off, %7, %8"
               : "=&v"(s.a), "=&v"(s.b), "=&v"(s.e)
               : "v"(lane * 16), "v"(lane * 4), "s"(xrs), "s"(tok * 1280), "s"(srs), "s"(S_ * 8) : "memory");
}
DEVI float wsum(float v) {
  v = sum8(v);
  v += dppf<0x140>(v);
  auto s = __builtin_amdgcn_permlane16_swap(__float_as_uint(v), __float_as_uint(v), false, false);
  v = __uint_as_float(s[0]) + __uint_as_float(s[1]);
  auto t = __builtin_amdgcn_permlane32_swap(__float_as_uint(v), __float_as_uint(v), false, false);
  return __uint_as_float(t[0]) + __uint_as_float(t[1]);
}
template <int N> DEVI u32 stat_pub(StatLd& s, rsrc_t srs, int tok, int lane) {
  asm volatile("s_waitcnt vmcnt(%3)" : "+v"(s.a), "+v"(s.b), "+v"(s.e) : "n"(N) : "memory");
  const u32 tag = s.e + TAGC;
  f32x2 e[5];
#pragma unroll
  for (int i = 0; i < 4; ++i) e[i] = f32x2{lo16(s.a[i]), hi16(s.a[i])};
  e[4] = f32x2{lo16(s.b), hi16(s.b)};
  f32x2 a = (e[0] + e[1]) + (e[2] + e[3]) + e[4];
  const float mean = wsum(a[0] + a[1]) * (1.f / 640.f);
  f32x2 q = bc2(0.f);
#pragma unroll
  for (int i = 0; i < 5; ++i) { const f32x2 d = e[i] - bc2(mean); q = fma2(d, d, q); }
  const float var = wsum(q[0] + q[1]) * (1.f / 640.f);
  const float den = __builtin_amdgcn_sqrtf(rbf(rbf(var) + 1e-5f));
  st64(u32x2{pk2(mean, den), tag}, srs, lane == 0 ? tok * 8 : OOB);
  return tag;
}
DEVI void rec_load(u32x2& r, rsrc_t srs, u32 voff) {
  asm volatile("buffer_load_dwordx2 %0, %1, %2, 0 offen sc1" : "=v"(r) : "v"(voff), "s"(srs) : "memory");
}

DEVI void touch(u32& d, rsrc_t rs, u32 voff, u32 soff) {
  asm volatile("buffer_load_dword %0, %1, %2, %3 offen" : "+v"(d) : "v"(voff), "s"(rs), "s"(soff) : "memory");
}

template <bool ATT> struct CS {
  f32x4 acc[6][4];
  bf16x8 S[4], H[2][4];
  u32 wa[4], xa[2];
  u32 wv[Cfg<ATT>::NW], xv[Cfg<ATT>::NX], ro[Cfg<ATT>::NR];
  u32x2 rec[Cfg<ATT>::NR];
  s16x4 rt[4];
  rsrc_t wrs, xrs, srs, ors, rrs;
  u32 pw, px, dummy, rv[2], rdst, wdst, xdst, ra, rso, rso2, tag;
  uint64_t xmask, bad;
};
template <bool ATT, int I, int P> DEVI void dma_w(CS<ATT>& t) {
  using C = Cfg<ATT>;
  dma16<(I & 3) * C::WSLOT + P * 1024>(t.wrs, t.wv[P], I * C::WSTEP, t.wdst);
}
template <bool ATT, int I, int P> DEVI void dma_x(CS<ATT>& t) {
  using C = Cfg<ATT>;
  if constexpr (P + 1 < C::NX) dma16<(I % 3) * C::XSLOT + P * 1024>(t.xrs, t.xv[P], I * 128, t.xdst);
  else dma16m<(I % 3) * C::XSLOT + P * 1024>(t.xrs, t.xv[P], I * 128, t.xdst, t.xmask);
}
template <bool ATT, int I> DEVI void dma_wall(CS<ATT>& t) {
  dma_w<ATT, I, 0>(t); dma_w<ATT, I, 1>(t);
  if constexpr (!ATT) dma_w<ATT, I, 2>(t);
}
template <bool ATT, int I> DEVI void dma_xall(CS<ATT>& t) {
  dma_x<ATT, I, 0>(t); dma_x<ATT, I, 1>(t);
  if constexpr (ATT) dma_x<ATT, I, 2>(t);
}
template <bool ATT> DEVI void rec_wait(CS<ATT>& t) {
  using C = Cfg<ATT>;
  if constexpr (ATT) asm volatile("s_waitcnt vmcnt(0)" : "+v"(t.rec[0]), "+v"(t.rec[1]), "+v"(t.rec[2]), "+v"(t.rec[3]), "+v"(t.rec[4]), "+v"(t.rec[5]), "+v"(t.dummy) :: "memory");
  else asm volatile("s_waitcnt vmcnt(0)" : "+v"(t.rec[0]), "+v"(t.rec[1]), "+v"(t.rec[2]), "+v"(t.rec[3]), "+v"(t.dummy) :: "memory");
}
template <bool ATT> DEVI void rec_test(CS<ATT>& t) {
  rec_wait<ATT>(t);
  u32 bad = 0;
#pragma unroll
  for (int i = 0; i < Cfg<ATT>::NR; ++i) bad |= t.rec[i][1] ^ t.tag;
  t.bad = __builtin_amdgcn_ballot_w64(bad != 0);
}
template <bool ATT> DEVI void rec_retry(CS<ATT>& t) {
#pragma unroll
  for (int i = 0; i < 6; ++i) asm volatile("" : "+v"(t.acc[i][0]), "+v"(t.acc[i][1]), "+v"(t.acc[i][2]), "+v"(t.acc[i][3]));
  while (__builtin_expect(t.bad != 0, 0)) {
    __builtin_amdgcn_s_sleep(2);
    rec_load(t.rec[0], t.srs, t.ro[0]); rec_load(t.rec[1], t.srs, t.ro[1]);
    rec_load(t.rec[2], t.srs, t.ro[2]); rec_load(t.rec[3], t.srs, t.ro[3]);
    if constexpr (ATT) { rec_load(t.rec[4], t.srs, t.ro[4]); rec_load(t.rec[5], t.srs, t.ro[5]); }
    rec_test<ATT>(t);
  }
}
template <bool ATT, int Q> DEVI void piece(CS<ATT>& t) {
  using C = Cfg<ATT>;
  constexpr int ty = C::SC.ty[Q], I = C::SC.idx[Q], P = C::SC.sub[Q];
  if constexpr (ty == T_X) dma_x<ATT, I, P>(t);
  if constexpr (ty == T_W) dma_w<ATT, I, P>(t);
  if constexpr (ty == T_R) dma16<P * 1024>(t.rrs, t.rv[P], 0, t.rdst);
  if constexpr (ty == T_PW) touch(t.dummy, t.wrs, t.pw, P * (C::WSTEP / 32));
  if constexpr (ty == T_PX) touch(t.dummy, t.xrs, t.px, P);
  if constexpr (ty == T_REC) rec_load(t.rec[P], t.srs, t.ro[P]);
}
template <bool ATT, int ST, int Q> DEVI void hook(CS<ATT>& t, AS3 char* sm) {
  using C = Cfg<ATT>;
  constexpr int M = Q >= 14 ? ST + 1 : ST, R = Q >= 14 ? Q - 14 : Q + 10;
  if constexpr (C::at(M, R) >= 0) piece<ATT, C::at(M, R)>(t);
  if constexpr (M == 19 && R == 11) { t.rt[0] = ldtr(sm + t.ra); t.rt[1] = ldtr(sm + t.ra + 256); }
  if constexpr (M == 19 && R == 13) { t.rt[2] = ldtr(sm + t.ra + 8); t.rt[3] = ldtr(sm + t.ra + 264); }
  if constexpr (M == 19 && R == 19) st128(__builtin_bit_cast(u32x4, cat(t.rt[0], t.rt[1])), t.ors, t.rso);
  if constexpr (M == 19 && R == 21) st128(__builtin_bit_cast(u32x4, cat(t.rt[2], t.rt[3])), t.ors, t.rso2);
}
template <bool ATT, int ST, int I> DEVI bf16x8 rd_w(CS<ATT>& t, AS3 char* sm) {
  if constexpr (ATT) {
    constexpr int o = (ST & 3) * 16384;
    return cat(ldtr(sm + t.wa[I] + o), ldtr(sm + t.wa[I] + o + 2048));
  } else {
    constexpr int o = (ST & 1) * 24576 + (I & 1) * 12288 + (I >> 1) * 128;
    return cat(ldtr(sm + t.wa[(ST >> 1) & 1] + o), ldtr(sm + t.wa[(ST >> 1) & 1] + o + 1536));
  }
}
template <bool ATT, int ST, int I> DEVI bf16x8 rd_x(CS<ATT>& t, AS3 char* sm) {
  return ld128(sm + t.xa[ST & 1] + ((ST >> 1) % 3) * Cfg<ATT>::XSLOT + I * 2048);
}
template <bool ATT, int ST, int I> DEVI void rdS(CS<ATT>& t, AS3 char* sm) {
  if constexpr (ST < 20) {
    if constexpr (ATT) t.S[(6 * ST + I) & 3] = rd_x<ATT, ST, I>(t, sm);
    else t.S[(6 * ST + I) & 3] = rd_w<ATT, ST, I>(t, sm);
  }
}
template <bool ATT, int ST, int I> DEVI void rdH(CS<ATT>& t, AS3 char* sm) {
  if constexpr (ST < 20) {
    if constexpr (ATT) t.H[ST & 1][I] = rd_w<ATT, ST, I>(t, sm);
    else t.H[ST & 1][I] = rd_x<ATT, ST, I>(t, sm);
  }
}
template <bool ATT, int ST, int I, int J> DEVI void mm(CS<ATT>& t, AS3 char* sm) {
  sched();
  {
  if constexpr (ATT) t.acc[I][J] = mfma(t.H[ST & 1][J], t.S[(6 * ST + I) & 3], t.acc[I][J]);
  else t.acc[I][J] = mfma(t.S[(6 * ST + I) & 3], t.H[ST & 1][J], t.acc[I][J]);
  }
  sched();
  hook<ATT, ST, I * 4 + J>(t, sm);
  sched();
}
template <bool A, int ST> DEVI void stage(CS<A>& t, AS3 char* sm) {
  mm<A, ST, 0, 0>(t, sm); rdS<A, ST, 3>(t, sm); mm<A, ST, 0, 1>(t, sm); mm<A, ST, 0, 2>(t, sm); mm<A, ST, 0, 3>(t, sm);
  mm<A, ST, 1, 0>(t, sm); rdS<A, ST, 4>(t, sm); mm<A, ST, 1, 1>(t, sm); mm<A, ST, 1, 2>(t, sm); mm<A, ST, 1, 3>(t, sm);
  mm<A, ST, 2, 0>(t, sm); rdS<A, ST, 5>(t, sm); mm<A, ST, 2, 1>(t, sm); mm<A, ST, 2, 2>(t, sm); mm<A, ST, 2, 3>(t, sm);
  mm<A, ST, 3, 0>(t, sm); mm<A, ST, 3, 1>(t, sm);
  if constexpr (ST < 19) { wait_lgkm<0>(); wait_vm<Cfg<A>::vm(ST)>(); bar(); sched(); }
  rdS<A, ST + 1, 0>(t, sm); mm<A, ST, 3, 2>(t, sm); rdH<A, ST + 1, 0>(t, sm); mm<A, ST, 3, 3>(t, sm); rdH<A, ST + 1, 1>(t, sm);
  mm<A, ST, 4, 0>(t, sm); rdS<A, ST + 1, 1>(t, sm); mm<A, ST, 4, 1>(t, sm); rdH<A, ST + 1, 2>(t, sm);
  mm<A, ST, 4, 2>(t, sm); rdH<A, ST + 1, 3>(t, sm); mm<A, ST, 4, 3>(t, sm);
  mm<A, ST, 5, 0>(t, sm); rdS<A, ST + 1, 2>(t, sm); mm<A, ST, 5, 1>(t, sm); mm<A, ST, 5, 2>(t, sm); mm<A, ST, 5, 3>(t, sm);
  if constexpr (ST == 18) { rec_test<A>(t); sched(); }
  if constexpr (ST < 19) stage<A, ST + 1>(t, sm);
}

struct Ptrs { const char *x, *w0, *w1, *wo, *wd; char *a2f, *w2f, *st; };

template <bool ATT> DEVI void k1_main(CS<ATT>& t, int w, int mt, int ns, const Ptrs& P, AS3 char* sm) {
  using C = Cfg<ATT>;
  const int lane = threadIdx.x & 63, wh = w >> 2, wq = w & 3;
  const int unit = blockIdx.x * 8 + w;
  t.xrs = mkrs(P.x, S_ * D_ * 2);
  t.srs = mkrs(P.st, S_ * 8 + 4);
  StatLd sl;
  stat_load(sl, t.xrs, t.srs, unit, lane);
#pragma unroll
  for (int i = 0; i < C::NX; ++i) {
    const int r = (wq * C::NX + i) * 8 + (lane >> 3);
    int tok;
    if (ATT) tok = 2 * min(max(mt * UT_ATT - 7 + r, 0), 1023) + wh;
    else tok = tok_of(min((2 * mt + wh) * HT_MLP + r, S_ - 1));
    t.xv[i] = tok * 1280 + (((lane & 7) ^ ((r >> 1) & 7)) << 4);
  }
  t.xdst = (u32)(size_t)sm + C::XR + w * (C::NX * 1024);
  t.xmask = wq == 3 ? 0x00ffffffffffffffull : ~0ull;
  dma_xall<ATT, 0>(t);
  if (ATT) {
    t.wrs = mkrs(P.w0 + ns * 160, D_ * 3 * D_ * 2 - ns * 160);
#pragma unroll
    for (int i = 0; i < C::NW; ++i) {
      int row = 4 * w + 2 * i + (lane >> 5);
      int rho = (row & 3) + 4 * ((row >> 3) & 1);
      int c = ((lane & 31) - 2 * rho) & 31;
      int so = c < 10 ? 2560 + c * 16 : (c == 10 || c == 31) ? 0 : c <= 20 ? (c - 11) * 16 : 1280 + (c - 21) * 16;
      t.wv[i] = row * 3840 + so;
    }
  } else {
    t.wrs = mkrs((wh ? P.w1 : P.w0) + ns * 384, D_ * F_ * 2 - ns * 384);
#pragma unroll
    for (int i = 0; i < C::NW; ++i) {
      int Q = i * 64 + lane;
      int r = Q / 24, c = Q - r * 24, row = wq * 8 + r;
      int u = (c >> 1) ^ ((row >> 1) & 1) ^ ((row >> 2) & 2);
      t.wv[i] = row * 3456 + u * 32 + (c & 1) * 16;
    }
  }
  t.wdst = (u32)(size_t)sm + w * (C::NW * 1024);
  dma_wall<ATT, 0>(t); dma_wall<ATT, 1>(t);
  sched();
  {
    const int xcd = blockIdx.x & 7, idx = blockIdx.x >> 3, xn = xcd & 3;
    const int id = ATT ? (idx * 8 + w) * 18 + lane : ((idx - 12) * 4 + wq) * 24 + lane;
    const int r = id / 9, l = id - r * 9;
    const int col = ATT ? ((xn * 320) & ~127) - ns * 160 + (l / 3) * (1280 - 384) + l * 128 : (2 * xn - ns) * 384 + l * 128;
    t.pw = (lane < (ATT ? 18 : 24) && r < 192) ? (128 + r) * (C::WSTEP / 32) + col : OOB;
    t.px = (lane < 20 && idx < 30) ? ((xcd >> 2) * 848 + (idx * 8 + w) * 5 + (lane >> 2)) * 1280 + (3 + (lane & 3)) * 128 : OOB;
    t.dummy = 0;
  }
  {
    const int kb = unit / 24, r24 = unit - kb * 24, ni = r24 / 3, part = r24 - ni * 3;
    const bool has = kb < KB2;
    t.rrs = kb < 20 ? mkrs(P.wo, D_ * D_ * 2) : mkrs(P.wd, F_ * D_ * 2);
    const int kl = (kb + 4) % 6, cs = kb < 16 ? 0 : kb < 20 ? 1 : kl < 4 ? 2 : 3;
    const int base = cs == 0 ? 40 * kb : cs == 1 ? 160 * kb - 2544 : cs == 2 ? (kb - 20) * 32 - 16 * kl : (kb - 20) * 32;
    const int c2 = (0x04401404 >> (cs * 8)) & 255, c3 = (0x08042808 >> (cs * 8)) & 255, c4 = (0x10085014 >> (cs * 8)) & 255;
    const int row = base + ((lane >> 2) & 3) + c2 * ((lane >> 4) & 1) + c3 * (lane >> 5);
    t.rv[0] = has ? row * 1280 + ni * 160 + part * 64 + (lane & 3) * 16 : OOB;
    t.rv[1] = t.rv[0] + c4 * 1280;
    t.rdst = (u32)(size_t)sm + C::RT + w * 2048;
    t.ors = mkrso(P.w2f, W2F_BYTES, has ? (ni * KB2 + kb) * 5120 + part * 2048 : 0);
    t.rso = has ? lane * 16 : OOB;
    t.rso2 = has && part < 2 ? lane * 16 + 1024 : OOB;
    t.ra = C::RT + w * 2048 + (8 * (lane >> 4) + ((lane >> 2) & 3)) * 64 + (lane & 3) * (part == 2 ? 8 : 16);
  }
  sched();
  if (wq == 3 && lane < 24) {
    u32x4 ones = {0x3f803f80u, 0x3f803f80u, 0x3f803f80u, 0x3f803f80u};
    *(AS3 u32x4*)(sm + C::XR + (lane >> 3) * C::XSLOT + (w + 1) * (C::NX * 1024) - 128 + (lane & 7) * 16) = ones;
  }
#pragma unroll
  for (int i = 0; i < C::NR; ++i) {
    const int r = 16 * i + (lane & 15);
    int tok;
    if (ATT) tok = 2 * min(max(mt * UT_ATT - 7 + r, 0), 1023) + wh;
    else tok = tok_of(min((2 * mt + wh) * HT_MLP + r, S_ - 1));
    t.ro[i] = tok * 8;
  }
#pragma unroll
  for (int i = 0; i < 6; ++i)
#pragma unroll
    for (int c = 0; c < 4; ++c) t.acc[i][c] = f32x4{0.f, 0.f, 0.f, 0.f};
  t.tag = stat_pub<2 * C::NW + C::NX>(sl, t.srs, unit, lane);
  sched();
  wait_lgkm<0>();
  wait_vm<C::vm(-1)>();
  bar();
  sched();
  rdS<ATT, 0, 0>(t, sm); rdH<ATT, 0, 0>(t, sm); rdH<ATT, 0, 1>(t, sm); rdH<ATT, 0, 2>(t, sm); rdH<ATT, 0, 3>(t, sm);
  rdS<ATT, 0, 1>(t, sm); rdS<ATT, 0, 2>(t, sm);
  stage<ATT, 0>(t, sm);
  rec_retry<ATT>(t);
  sched();
}

DEVI void k1_mlp(int w, int mt, int fs, const Ptrs& P, AS3 char* sm) {
  char* a2f = P.a2f;
  using C = Cfg<false>;
  const int lane = threadIdx.x & 63, wm = w >> 2, wn = w & 3;
  const int G = lane >> 4, tq = lane & 15;
  CS<false> t;
  {
    int q = (lane >> 2) & 3, p = lane & 3;
    t.wa[0] = opq((8 * G + q) * 384 + ((wn ^ (q >> 1) ^ ((G & 1) << 1)) << 5) + 8 * p);
    t.wa[1] = opq(t.wa[0] + 49152);
    t.xa[0] = opq(C::XR + (wm * 64 + tq) * 128 + ((G ^ (tq >> 1)) << 4));
    t.xa[1] = opq(t.xa[0] ^ 64);
  }
  k1_main<false>(t, w, mt, fs, P, sm);
  f32x4 rc[4];
#pragma unroll
  for (int i = 0; i < 4; ++i) {
    const float den = hi16(t.rec[i][0]), rs = __builtin_amdgcn_rcpf(den);
    rc[i] = f32x4{-lo16(t.rec[i][0]), -2.30220820f * rs, -0.10294324f * rs * rs * rs, den * den};
  }
  u32 vo[4];
#pragma unroll
  for (int i = 0; i < 4; ++i) {
    const int r = i * 16 + tq, sg = (2 * mt + wm) * HT_MLP + r;
    vo[i] = (r < HT_MLP && sg < S_) ? a2f_off(sg) : OOB;
  }
  const rsrc_t ors = mkrso(a2f, A2F_BYTES, (20 + fs * 6 + wn) * 4096);
  const rsrc_t ors2 = mkrso(a2f, A2F_BYTES, (24 + fs * 6 + (wn >> 1)) * 4096 + (wn & 1) * 512);
  u32 hp[3][4][2];
#pragma unroll
  for (int b = 0; b < 3; ++b) {
    f32x4 cg, cu;
#pragma unroll
    for (int r = 0; r < 4; ++r) { cg[r] = dppf<0x15F>(t.acc[2 * b][3][r]); cu[r] = dppf<0x15F>(t.acc[2 * b + 1][3][r]); }
#pragma unroll
    for (int i = 0; i < 4; ++i) {
      const f32x2 nm = bc2(rc[i][0]), ca = bc2(rc[i][1]), cb = bc2(rc[i][2]), cd = bc2(rc[i][3]);
#pragma unroll
      for (int e = 0; e < 2; ++e) {
        f32x2 g = fma2(e ? half2<1>(cg) : half2<0>(cg), nm, e ? half2<1>(t.acc[2 * b][i]) : half2<0>(t.acc[2 * b][i]));
        f32x2 u = fma2(e ? half2<1>(cu) : half2<0>(cu), nm, e ? half2<1>(t.acc[2 * b + 1][i]) : half2<0>(t.acc[2 * b + 1][i]));
        f32x2 z = g * fma2(g * g, cb, ca);
        f32x2 ex = {__builtin_amdgcn_exp2f(z[0]), __builtin_amdgcn_exp2f(z[1])};
        f32x2 d = fma2(ex, cd, cd);
        f32x2 rd = {__builtin_amdgcn_rcpf(d[0]), __builtin_amdgcn_rcpf(d[1])};
        hp[b][i][e] = pk2v(g * u * rd);
      }
      if (b == 1) st128(u32x4{hp[0][i][0], hp[0][i][1], hp[1][i][0], hp[1][i][1]}, ors, vo[i] + G * 256);
      if (b == 2 && (i & 1))
        st128(pair_rows(hp[2][i - 1][0], hp[2][i - 1][1], hp[2][i][0], hp[2][i][1]), ors2, ((G & 1) ? vo[i] : vo[i - 1]) + (G >> 1) * 256);
    }
  }
}

DEVI void k1_att(int w, int mt, int hg, const Ptrs& P, AS3 char* sm) {
  char* a2f = P.a2f;
  using C = Cfg<true>;
  const int lane = threadIdx.x & 63, hl = w & 3, par = w >> 2;
  const int G = lane >> 4, tq = lane & 15, q = (lane >> 2) & 3, p = lane & 3;
  CS<true> t;
  {
    int rho = q + 4 * (G & 1);
    int rowb = (8 * G + q) * 512;
    int hb = hl * 40;
    int o0 = 176 + 8 * p, o1 = 336 + 8 * p;
    int o2 = p == 0 ? 176 + 32 : p == 1 ? 0 : p == 2 ? 8 : 24;
    int o3 = p == 0 ? 336 + 32 : p == 1 ? 32 : p == 2 ? 16 : 8;
    t.wa[0] = opq(rowb + ((o0 + hb + 32 * rho) & 511));
    t.wa[1] = opq(rowb + ((o1 + hb + 32 * rho) & 511));
    t.wa[2] = opq(rowb + ((o2 + hb + 32 * rho) & 511));
    t.wa[3] = opq(rowb + ((o3 + hb + 32 * rho) & 511));
    t.xa[0] = opq(C::XR + (par * 96 + tq) * 128 + ((G ^ (tq >> 1)) << 4));
    t.xa[1] = opq(t.xa[0] ^ 64);
  }
  k1_main<true>(t, w, mt, hg, P, sm);
  f32x2 ms[6];
#pragma unroll
  for (int T = 0; T < 6; ++T) ms[T] = f32x2{-lo16(t.rec[T][0]), __builtin_amdgcn_rcpf(hi16(t.rec[T][0]))};
  f32x4 cs[4];
#pragma unroll
  for (int c = 0; c < 4; ++c)
#pragma unroll
    for (int r = 0; r < 4; ++r) cs[c][r] = dppf<0x15F>(t.acc[5][c][r]);
  AS3 char* vs = sm + w * 6144;
  const u32 g0m = G == 0 ? ~0u : 0u;
  u32x4 qf[6], kf[6];
#pragma unroll
  for (int T = 0; T < 6; ++T) {
    const f32x2 nm = bc2(ms[T][0]), rs = bc2(ms[T][1]), r2 = bc2(G == 0 ? 1.f : ms[T][1]);
    u32 c00 = pk2v(fma2(half2<0>(cs[0]), nm, half2<0>(t.acc[T][0]))), c01 = pk2v(fma2(half2<1>(cs[0]), nm, half2<1>(t.acc[T][0])));
    u32 c10 = pk2v(fma2(half2<0>(cs[1]), nm, half2<0>(t.acc[T][1])) * rs), c11 = pk2v(fma2(half2<1>(cs[1]), nm, half2<1>(t.acc[T][1])) * rs);
    u32 c20 = pk2v(fma2(half2<0>(cs[2]), nm, half2<0>(t.acc[T][2])) * r2), c21 = pk2v(fma2(half2<1>(cs[2]), nm, half2<1>(t.acc[T][2])) * r2);
    u32 c30 = pk2v(fma2(half2<0>(cs[3]), nm, half2<0>(t.acc[T][3])) * rs), c31 = pk2v(fma2(half2<1>(cs[3]), nm, half2<1>(t.acc[T][3])) * rs);
    qf[T] = u32x4{c00, c01, c20 & g0m, c21 & g0m};
    kf[T] = u32x4{c10, c11, c30 & g0m, c31 & g0m};
    *(AS3 u32x4*)(vs + (16 * T + tq) * 64 + ((G ^ ((tq >> 1) & 3)) << 4)) = u32x4{c20, c21, c30, c31};
  }
  int va0, va1;
  {
    int row = 4 * G + q;
    int g0 = p == 0 ? 1 : p == 3 ? 3 : 2, h0 = p == 2 ? 8 : 0;
    va0 = row * 64 + ((g0 ^ ((row >> 1) & 3)) << 4) + h0;
    va1 = row * 64 + ((1 ^ ((row >> 1) & 3)) << 4) + 8;
  }
  const float NEG = -__builtin_inff();
  const f32x4 z4 = {0.f, 0.f, 0.f, 0.f};
  bool oka[4], ok0[4];
  f32x4 bb;
#pragma unroll
  for (int r = 0; r < 4; ++r) {
    const int key = 4 * G + r;
    oka[r] = key <= tq && key >= tq - 7;
    ok0[r] = oka[r] && (mt > 0 || key >= 7);
    bb[r] = key >= tq + 9 ? 0.f : NEG;
  }
  const u32 ma0 = (oka[0] ? 0xffffu : 0u) | (oka[1] ? 0xffff0000u : 0u), ma1 = (oka[2] ? 0xffffu : 0u) | (oka[3] ? 0xffff0000u : 0u);
  const u32x4 ones = {0x3f803f80u, 0x3f803f80u, 0x3f803f80u, 0x3f803f80u};
  const float qs = 0.22360679775f * 1.44269504089f;
  const int H = hg * 4 + hl, u0 = mt * UT_ATT - 7;
  const rsrc_t ors = mkrso(a2f, A2F_BYTES, (H >> 1) * 4096 + (H & 1) * 512);
  const rsrc_t ors2 = mkrso(a2f, A2F_BYTES, (16 + (H >> 3)) * 4096 + ((H >> 1) & 3) * 256 + (H & 1) * 8);
  u32x4 pf[6];
#pragma unroll
  for (int T = 0; T < 6; ++T) {
    f32x4 sa = mfmau(kf[T], qf[T], z4), s;
    if (T > 0) {
      f32x4 sb = mfmau(kf[T > 0 ? T - 1 : 0], qf[T], bb);
#pragma unroll
      for (int r = 0; r < 4; ++r) s[r] = oka[r] ? sa[r] : sb[r];
    } else {
#pragma unroll
      for (int r = 0; r < 4; ++r) s[r] = ok0[r] ? sa[r] : NEG;
    }
    const float mx = xg_max(fmaxf(fmaxf(s[0], s[1]), fmaxf(s[2], s[3])));
    const float c = ms[T][1] * qs;
    const f32x2 e0 = fma2(half2<0>(s), bc2(c), bc2(-mx * c)), e1 = fma2(half2<1>(s), bc2(c), bc2(-mx * c));
    const u32 p0 = pk2(__builtin_amdgcn_exp2f(e0[0]), __builtin_amdgcn_exp2f(e0[1]));
    const u32 p1 = pk2(__builtin_amdgcn_exp2f(e1[0]), __builtin_amdgcn_exp2f(e1[1]));
    pf[T] = u32x4{p0 & ma0, p1 & ma1, p0 & ~ma0, p1 & ~ma1};
  }
  s16x4 vp0 = {0, 0, 0, 0}, vp1 = {0, 0, 0, 0};
  u32 oa[2][2], ob[2][2];
#pragma unroll
  for (int T = 0; T < 6; ++T) {
    s16x4 vc0 = ldtr(vs + va0 + T * 1024), vc1 = ldtr(vs + va1 + T * 1024);
    const bf16x8 pb = __builtin_bit_cast(bf16x8, pf[T]);
    f32x4 sm4 = mfma(__builtin_bit_cast(bf16x8, ones), pb, z4);
    f32x4 o0 = mfma(cat(vc0, vp0), pb, z4);
    f32x4 o1 = mfma(cat(vc1, vp1), pb, z4);
    vp0 = vc0; vp1 = vc1;
    const f32x2 inv = bc2(__builtin_amdgcn_rcpf(sm4[0]));
    oa[T & 1][0] = pk2v(half2<0>(o0) * inv); oa[T & 1][1] = pk2v(half2<1>(o0) * inv);
    ob[T & 1][0] = pk2v(half2<0>(o1) * inv); ob[T & 1][1] = pk2v(half2<1>(o1) * inv);
    if (T & 1) {
      const int i = 16 * (T - 1 + (G & 1)) + tq, u = u0 + i;
      const u32 vo = (i >= 7 && i < 7 + UT_ATT && u < 1024) ? a2f_off((u >> 4) * 32 + par * 16 + (u & 15)) : OOB;
      st128(pair_rows(oa[0][0], oa[0][1], oa[1][0], oa[1][1]), ors, vo + (G >> 1) * 256);
      st64((G & 1) ? u32x2{ob[1][0], ob[1][1]} : u32x2{ob[0][0], ob[0][1]}, ors2, G < 2 ? vo : OOB);
    }
  }
}

__global__ __launch_bounds__(512) void k1(const char* __restrict__ x, const char* __restrict__ wqkv,
                                          const char* __restrict__ wo, const char* __restrict__ wg,
                                          const char* __restrict__ wu, const char* __restrict__ wd,
                                          char* __restrict__ a2f, char* __restrict__ w2f, char* __restrict__ st) {
  __shared__ __attribute__((aligned(1024))) char smem[LDS_K1];
  AS3 char* sm = (AS3 char*)smem;
  const int b = blockIdx.x, xcd = b & 7, idx = b >> 3;
  const int xm = xcd >> 2, xn = xcd & 3;
  const int w = __builtin_amdgcn_readfirstlane(threadIdx.x >> 6);
  unsigned long long* tm = (unsigned long long*)(st + 65536);
  const unsigned long long t0 = __builtin_amdgcn_s_memrealtime();
#define FIN do { if ((threadIdx.x & 63) == 0) { tm[(b * 8 + w) * 2] = t0; tm[(b * 8 + w) * 2 + 1] = __builtin_amdgcn_s_memrealtime(); } } while (0)
  if (idx < 12) {
    const int mt = xm * 6 + (idx >> 1), hg = xn * 2 + (idx & 1);
    const Ptrs P = {x, wqkv, wqkv, wo, wd, a2f, w2f, st};
    k1_att(w, mt, hg, P, sm);
    FIN;
    return;
  }
  const int j = idx - 12;
  int mt, fs;
  if (xm == 0) {
    const int q = xn * 20 + j;
    mt = q % 9; fs = q / 9;
  } else {
    if (j >= (xn == 3 ? 19 : 18)) {
      StatLd sl;
      stat_load(sl, mkrs(x, S_ * D_ * 2), mkrs(st, S_ * 8 + 4), b * 8 + w, threadIdx.x & 63);
      stat_pub<0>(sl, mkrs(st, S_ * 8 + 4), b * 8 + w, threadIdx.x & 63);
      FIN;
      return;
    }
    const int e = xn * 18 + j;
    mt = e < 72 ? 9 + (e & 7) : 8; fs = e < 72 ? e >> 3 : 8;
  }
  const Ptrs P = {x, wg, wu, wo, wd, a2f, w2f, st};
  k1_mlp(w, mt, fs, P, sm);
  FIN;
}

constexpr int K2P = 6;
struct K2State {
  f32x4 acc[4][5];
  bf16x8 a[K2P][4], wv[K2P][5];
  const char* pa; const char* pw;
};
template <int I> DEVI void k2_load(K2State& t) {
#pragma unroll
  for (int i = 0; i < 4; ++i) t.a[I % K2P][i] = *(const bf16x8*)(t.pa + (size_t)I * 16384 + i * 1024);
#pragma unroll
  for (int i = 0; i < 5; ++i) t.wv[I % K2P][i] = *(const bf16x8*)(t.pw + (size_t)I * 20480 + i * 1024);
}
template <int I> DEVI void k2_mfma(K2State& t) {
#pragma unroll
  for (int n = 0; n < 5; ++n)
#pragma unroll
    for (int i = 0; i < 4; ++i)
      t.acc[i][n] = __builtin_amdgcn_mfma_f32_16x16x32_bf16(t.wv[I % K2P][n], t.a[I % K2P][i], t.acc[i][n], 0, 0, 0);
}
template <int I> DEVI void k2_step(K2State& t, bool extra) {
  k2_mfma<I>(t);
  sched();
  if constexpr (I + K2P < 18) k2_load<I + K2P>(t);
  if constexpr (I + K2P == 18) { if (extra) k2_load<18>(t); }
  sched();
  if constexpr (I < 17) k2_step<I + 1>(t, extra);
}
template <int I> DEVI void k2_pro(K2State& t) {
  k2_load<I>(t);
  if constexpr (I + 1 < K2P) k2_pro<I + 1>(t);
}

__global__ __launch_bounds__(256, 1) void k2(const char* __restrict__ a2f, const char* __restrict__ w2f,
                                             const char* __restrict__ x, char* __restrict__ out, u32* __restrict__ ep) {
  __shared__ __attribute__((aligned(1024))) char smem[4 * 4 * 5 * 1024];
  AS3 char* sm = (AS3 char*)smem;
  const int b = blockIdx.x, xcd = b & 7, idx = b >> 3;
  const int mi = (xcd >> 1) * 8 + (idx >> 2), ni = (xcd & 1) * 4 + (idx & 3);
  const int lane = threadIdx.x & 63, w = __builtin_amdgcn_readfirstlane(threadIdx.x >> 6);
  K2State t;
  const unsigned long long t0 = __builtin_amdgcn_s_memrealtime();
  t.pa = a2f + (size_t)(mi * KB2 + w) * 4096 + lane * 16;
  t.pw = w2f + (size_t)(ni * KB2 + w) * 5120 + lane * 16;
  k2_pro<0>(t);
  const int tok = tok_of(mi * 64 + w * 16 + (lane & 15)), G = lane >> 4;
  const size_t ro = (size_t)tok * 1280 + ni * 160;
  u32x4 xr0 = *(const u32x4*)(x + ro + G * 16), xr1 = *(const u32x4*)(x + ro + 64 + G * 16);
  u32x2 xr2 = *(const u32x2*)(x + ro + 128 + G * 8);
#pragma unroll
  for (int i = 0; i < 4; ++i)
#pragma unroll
    for (int n = 0; n < 5; ++n) t.acc[i][n] = f32x4{0.f, 0.f, 0.f, 0.f};
  sched();
  const bool extra = w < 2;
  k2_step<0>(t, extra);
  if (extra) k2_mfma<18>(t);
#pragma unroll
  for (int i = 0; i < 4; ++i)
#pragma unroll
    for (int n = 0; n < 5; ++n) *(AS3 f32x4*)(sm + ((i * 4 + w) * 5 + n) * 1024 + lane * 16) = t.acc[i][n];
  __syncthreads();
  f32x4 r[5];
#pragma unroll
  for (int n = 0; n < 5; ++n) {
    r[n] = *(AS3 f32x4*)(sm + ((w * 4 + 0) * 5 + n) * 1024 + lane * 16);
#pragma unroll
    for (int s = 1; s < 4; ++s) r[n] += *(AS3 f32x4*)(sm + ((w * 4 + s) * 5 + n) * 1024 + lane * 16);
  }
  u32x4 o0 = {pk2(r[0][0] + lo16(xr0[0]), r[0][1] + hi16(xr0[0])), pk2(r[0][2] + lo16(xr0[1]), r[0][3] + hi16(xr0[1])),
              pk2(r[1][0] + lo16(xr0[2]), r[1][1] + hi16(xr0[2])), pk2(r[1][2] + lo16(xr0[3]), r[1][3] + hi16(xr0[3]))};
  u32x4 o1 = {pk2(r[2][0] + lo16(xr1[0]), r[2][1] + hi16(xr1[0])), pk2(r[2][2] + lo16(xr1[1]), r[2][3] + hi16(xr1[1])),
              pk2(r[3][0] + lo16(xr1[2]), r[3][1] + hi16(xr1[2])), pk2(r[3][2] + lo16(xr1[3]), r[3][3] + hi16(xr1[3]))};
  u32x2 o2 = {pk2(r[4][0] + lo16(xr2[0]), r[4][1] + hi16(xr2[0])), pk2(r[4][2] + lo16(xr2[1]), r[4][3] + hi16(xr2[1]))};
  *(u32x4*)(out + ro + G * 16) = o0;
  *(u32x4*)(out + ro + 64 + G * 16) = o1;
  *(u32x2*)(out + ro + 128 + G * 8) = o2;
  if (b == 0 && threadIdx.x == 0) *ep += TAGC;
  if (b == 0 && w == 0) {
    const unsigned long long t1 = __builtin_amdgcn_s_memrealtime();
    const unsigned long long* tm = (const unsigned long long*)((char*)ep - S_ * 8 + 65536);
    unsigned long long mn = ~0ull, mx = 0;
    for (int i = lane; i < 2048; i += 64) { mn = min(mn, tm[2 * i]); mx = max(mx, tm[2 * i + 1]); }
    for (int o = 32; o; o >>= 1) { mn = min(mn, (unsigned long long)__shfl_xor((long long)mn, o)); mx = max(mx, (unsigned long long)__shfl_xor((long long)mx, o)); }
    long long X = MEAS == 1 ? mx - mn : MEAS == 2 ? t0 - mx : MEAS == 3 ? t1 - t0 : 0;
    if (X < 0) X = 0;
    if (X > 10000) X = 10000;
    while ((long long)(__builtin_amdgcn_s_memrealtime() - t1) < 10 * X) __builtin_amdgcn_s_sleep(1);
  }
}

extern "C" void kernel_launch(void* const* d_in, const int* in_sizes, int n_in, void* d_out, int out_size,
                              void* d_ws, size_t ws_size, hipStream_t stream) {
  const char* x = (const char*)d_in[0];
  char* a2f = (char*)d_ws;
  char* w2f = a2f + A2F_BYTES;
  char* st = w2f + W2F_BYTES;
  k1<<<256, 512, 0, stream>>>(x, (const char*)d_in[1], (const char*)d_in[2], (const char*)d_in[4],
                              (const char*)d_in[5], (const char*)d_in[6], a2f, w2f, st);
  k2<<<256, 256, 0, stream>>>(a2f, w2f, x, (char*)d_out, (u32*)(st + S_ * 8));
}
```

```cpp
#include <hip/hip_runtime.h>
#include <stdint.h>

typedef unsigned short u16;
typedef unsigned int u32;
typedef __attribute__((ext_vector_type(8))) __bf16 bf16x8;
typedef __attribute__((ext_vector_type(2))) __bf16 bf16x2;
typedef __attribute__((ext_vector_type(4))) short s16x4;
typedef __attribute__((ext_vector_type(8))) short s16x8;
typedef __attribute__((ext_vector_type(2))) float f32x2;
typedef __attribute__((ext_vector_type(4))) float f32x4;
typedef __attribute__((ext_vector_type(4))) u32 u32x4;
typedef __attribute__((ext_vector_type(2))) u32 u32x2;
#define AS3 __attribute__((address_space(3)))
#define DEVI __device__ __forceinline__

constexpr int S_ = 2048, D_ = 640, F_ = 1728;
constexpr int KB2 = 74;
constexpr int A2F_BYTES = 32 * KB2 * 4096;
constexpr int W2F_BYTES = 8 * KB2 * 5120;
constexpr int HT_MLP = 61;
constexpr int UT_ATT = 86;
#define CPRIO 0
#define LPRIO 3
#define ST_AUX 16

template <int N> DEVI void wait_vm() { asm volatile("s_waitcnt vmcnt(%0)" ::"n"(N) : "memory"); }
template <int N> DEVI void wait_lgkm() { asm volatile("s_waitcnt lgkmcnt(%0)" ::"n"(N) : "memory"); }
DEVI void bar() { __builtin_amdgcn_s_barrier(); }
DEVI void sched() { __builtin_amdgcn_sched_barrier(0); }
DEVI u32 opq(u32 v) { asm volatile("" : "+v"(v)); return v; }

DEVI u32 pk2(float a, float b) {
  f32x2 v = {a, b};
  bf16x2 r = __builtin_convertvector(v, bf16x2);
  return __builtin_bit_cast(u32, r);
}
DEVI u32 pk2v(f32x2 v) { return __builtin_bit_cast(u32, __builtin_convertvector(v, bf16x2)); }
DEVI float rbf(float a) { return __uint_as_float(pk2(a, 0.f) << 16); }
DEVI float lo16(u32 v) { return __uint_as_float(v << 16); }
DEVI float hi16(u32 v) { return __uint_as_float(v & 0xffff0000u); }
DEVI f32x2 bc2(float a) { return f32x2{a, a}; }
DEVI f32x2 fma2(f32x2 a, f32x2 b, f32x2 c) { return __builtin_elementwise_fma(a, b, c); }
template <int I> DEVI f32x2 half2(f32x4 v) { return __builtin_shufflevector(v, v, 2 * I, 2 * I + 1); }

typedef __amdgpu_buffer_rsrc_t rsrc_t;
DEVI rsrc_t mkrs(const char* p, int bytes) { return __builtin_amdgcn_make_buffer_rsrc((void*)p, (short)0, bytes, 0x00020000); }
template <int LOFF> DEVI void dma16(rsrc_t rs, u32 voff, u32 soff, u32 lds) {
  asm volatile("s_add_u32 m0, %0, %4\n\ts_nop 0\n\tbuffer_load_dwordx4 %1, %2, %3 offen lds"
               :: "s"(lds), "v"(voff), "s"(rs), "s"(soff), "i"(LOFF) : "memory", "scc");
}
template <int LOFF> DEVI void dma16m(rsrc_t rs, u32 voff, u32 soff, u32 lds, uint64_t mask) {
  asm volatile("s_add_u32 m0, %0, %4\n\ts_mov_b64 exec, %5\n\tbuffer_load_dwordx4 %1, %2, %3 offen lds\n\ts_mov_b64 exec, -1"
               :: "s"(lds), "v"(voff), "s"(rs), "s"(soff), "i"(LOFF), "s"(mask) : "memory", "scc");
}
DEVI void st128(u32x4 v, rsrc_t rs, u32 voff) { __builtin_amdgcn_raw_buffer_store_b128(v, rs, voff, 0, ST_AUX); }
DEVI void st64(u32x2 v, rsrc_t rs, u32 voff) { __builtin_amdgcn_raw_buffer_store_b64(v, rs, voff, 0, ST_AUX); }
DEVI rsrc_t mkrso(const char* p, int bytes, int off) { return mkrs(p + off, bytes - off); }
DEVI bf16x8 ld128(AS3 char* p) { return *(AS3 bf16x8*)p; }
DEVI s16x4 ldtr(AS3 char* p) { return __builtin_amdgcn_ds_read_tr16_b64_v4i16((AS3 s16x4*)p); }
DEVI bf16x8 cat(s16x4 a, s16x4 b) {
  s16x8 r = __builtin_shufflevector(a, b, 0, 1, 2, 3, 4, 5, 6, 7);
  return __builtin_bit_cast(bf16x8, r);
}
DEVI f32x4 mfma(bf16x8 a, bf16x8 b, f32x4 c) { return __builtin_amdgcn_mfma_f32_16x16x32_bf16(a, b, c, 0, 0, 0); }
DEVI f32x4 mfmau(u32x4 a, u32x4 b, f32x4 c) { return mfma(__builtin_bit_cast(bf16x8, a), __builtin_bit_cast(bf16x8, b), c); }
template <int CTRL> DEVI float dppf(float v) {
  return __int_as_float(__builtin_amdgcn_update_dpp(0, __float_as_int(v), CTRL, 0xf, 0xf, true));
}
DEVI float sum8(float v) {
  v += dppf<0xB1>(v);
  v += dppf<0x4E>(v);
  v += dppf<0x141>(v);
  return v;
}
DEVI float xg_max(float x) {
  auto s = __builtin_amdgcn_permlane16_swap(__float_as_uint(x), __float_as_uint(x), false, false);
  x = fmaxf(__uint_as_float(s[0]), __uint_as_float(s[1]));
  auto t = __builtin_amdgcn_permlane32_swap(__float_as_uint(x), __float_as_uint(x), false, false);
  return fmaxf(__uint_as_float(t[0]), __uint_as_float(t[1]));
}
DEVI u32x4 pair_rows(u32 a0, u32 a1, u32 b0, u32 b1) {
  auto s = __builtin_amdgcn_permlane16_swap(a0, b0, false, false);
  auto t = __builtin_amdgcn_permlane16_swap(a1, b1, false, false);
  return u32x4{s[0], t[0], s[1], t[1]};
}
DEVI int tok_of(int sg) { return (sg & ~31) | ((sg & 15) << 1) | ((sg >> 4) & 1); }
DEVI u32 a2f_off(int sg) { return (sg >> 6) * (KB2 * 4096) + ((sg >> 4) & 3) * 1024 + (sg & 15) * 16; }
constexpr u32 OOB = 0x80000000u;

#define CAP 5
#define PLO 9
#define PHI 22
#define RINT 8
#define PFW0 0
#define PFW1 3
#define PFW2 7
#define PFX0 0
#define PFX1 4
enum { T_X, T_W, T_R, T_S, T_REC, T_PW, T_PX };
struct Sched { int n, ty[128], idx[128], sub[128], first[23], vm[21]; };
constexpr Sched mk_sched(int NW, int NX, int NR) {
  Sched s = {};
  int al[128] = {}, du[128] = {}, ty[128] = {}, ix[128] = {}, sb[128] = {}, n = 0;
  for (int j = 0; j < 10; ++j) {
    for (int p = 0; p < NX; ++p) { ty[n] = T_X; ix[n] = j; sb[n] = p; al[n] = j < 3 ? -1 : 2 * j - 4; du[n] = 2 * j - 1; ++n; }
    for (int i = 2 * j; i < 2 * j + 2; ++i)
      for (int p = 0; p < NW; ++p) { ty[n] = T_W; ix[n] = i; sb[n] = p; al[n] = i < 4 ? -1 : i - 3; du[n] = i - 1; ++n; }
  }
  int due[128] = {}, q = 0;
  for (int m = -1; m <= 20; ++m) {
    s.first[m + 1] = s.n;
    int c = 0;
    while (q < n && c < (m < 0 ? NX + 2 * NW : CAP) && al[q] <= m) {
      s.ty[s.n] = ty[q]; s.idx[s.n] = ix[q]; s.sub[s.n] = sb[q]; due[s.n] = du[q]; ++s.n; ++q; ++c;
    }
    if (m == -1) { s.ty[s.n] = T_S; due[s.n] = 99; ++s.n; }
    if (m == PFW0 || m == PFW1 || m == PFW2) { s.ty[s.n] = T_PW; s.sub[s.n] = m == PFW0 ? 0 : m == PFW1 ? 192 : 320; due[s.n] = 99; ++s.n; }
    if (m == PFX0 || m == PFX1) { s.ty[s.n] = T_PX; s.sub[s.n] = m == PFX0 ? 0 : 384; due[s.n] = 99; ++s.n; }
    if (m == RINT) for (int p = 0; p < 2; ++p) { s.ty[s.n] = T_R; s.sub[s.n] = p; due[s.n] = 18; ++s.n; }
    if (m == 17) for (int p = 0; p < NR; ++p) { s.ty[s.n] = T_REC; s.sub[s.n] = p; due[s.n] = 99; ++s.n; }
    if (m <= 19) {
      int need = 0;
      for (int k = 0; k < s.n; ++k) if (due[k] <= m) need = k + 1;
      s.vm[m + 1] = s.n - need;
    }
  }
  s.first[22] = s.n;
  return s;
}
template <bool ATT> struct Cfg {
  static constexpr int NW = ATT ? 2 : 3, NX = ATT ? 3 : 2;
  static constexpr int NR = ATT ? 6 : 4;
  static constexpr int WSLOT = NW * 8192, XSLOT = NX * 8192;
  static constexpr int XR = 4 * WSLOT, RT = XR + 3 * XSLOT;
  static constexpr int WSTEP = 32 * (ATT ? 3 * D_ * 2 : F_ * 2);
  static constexpr Sched SC = mk_sched(NW, NX, NR);
  static constexpr int vm(int m) { return SC.vm[m + 1]; }
  static constexpr int at(int m, int r) {
    const int f = SC.first[m + 1], c = SC.first[m + 2] - f;
    const int lo = m == 0 ? 10 : c > 4 ? 1 : PLO;
    for (int k = 0; k < c; ++k) if (lo + (c > 1 ? k * (PHI - lo) / (c - 1) : 0) == r) return f + k;
    return -1;
  }
};
constexpr int LDS_K1 = 163840;
constexpr u32 TAGC = 0x9E3779B9u;

struct StatLd { u32x4 a; u32 b, e; };
DEVI void stat_load(StatLd& s, rsrc_t xrs, rsrc_t srs, int tok, int lane) {
  asm volatile("buffer_load_dwordx4 %0, %3, %5, %6 offen\n\tbuffer_load_dword %1, %4, %5, %6 offen offset:1024\n\t"
               "buffer_load_dword %2, off, %7, %8"
               : "=&v"(s.a), "=&v"(s.b), "=&v"(s.e)
               : "v"(lane * 16), "v"(lane * 4), "s"(xrs), "s"(tok * 1280), "s"(srs), "s"(S_ * 8) : "memory");
}
DEVI float wsum(float v) {
  v = sum8(v);
  v += dppf<0x140>(v);
  auto s = __builtin_amdgcn_permlane16_swap(__float_as_uint(v), __float_as_uint(v), false, false);
  v = __uint_as_float(s[0]) + __uint_as_float(s[1]);
  auto t = __builtin_amdgcn_permlane32_swap(__float_as_uint(v), __float_as_uint(v), false, false);
  return __uint_as_float(t[0]) + __uint_as_float(t[1]);
}
template <int N> DEVI u32 stat_pub(StatLd& s, rsrc_t srs, int tok, int lane) {
  asm volatile("s_waitcnt vmcnt(%3)" : "+v"(s.a), "+v"(s.b), "+v"(s.e) : "n"(N) : "memory");
  const u32 tag = s.e + TAGC;
  f32x2 e[5];
#pragma unroll
  for (int i = 0; i < 4; ++i) e[i] = f32x2{lo16(s.a[i]), hi16(s.a[i])};
  e[4] = f32x2{lo16(s.b), hi16(s.b)};
  f32x2 a = (e[0] + e[1]) + (e[2] + e[3]) + e[4];
  const float mean = wsum(a[0] + a[1]) * (1.f / 640.f);
  f32x2 q = bc2(0.f);
#pragma unroll
  for (int i = 0; i < 5; ++i) { const f32x2 d = e[i] - bc2(mean); q = fma2(d, d, q); }
  const float var = wsum(q[0] + q[1]) * (1.f / 640.f);
  const float den = __builtin_amdgcn_sqrtf(rbf(rbf(var) + 1e-5f));
  st64(u32x2{pk2(mean, den), tag}, srs, lane == 0 ? tok * 8 : OOB);
  return tag;
}
DEVI void rec_load(u32x2& r, rsrc_t srs, u32 voff) {
  asm volatile("buffer_load_dwordx2 %0, %1, %2, 0 offen sc1" : "=v"(r) : "v"(voff), "s"(srs) : "memory");
}

DEVI void touch(u32& d, rsrc_t rs, u32 voff, u32 soff) {
  asm volatile("buffer_load_dword %0, %1, %2, %3 offen" : "+v"(d) : "v"(voff), "s"(rs), "s"(soff) : "memory");
}

template <bool ATT> struct CS {
  f32x4 acc[6][4];
  bf16x8 S[4], H[2][4];
  u32 wa[4], xa[2];
  u32 wv[Cfg<ATT>::NW], xv[Cfg<ATT>::NX], ro[Cfg<ATT>::NR];
  u32x2 rec[Cfg<ATT>::NR];
  s16x4 rt[4];
  rsrc_t wrs, xrs, srs, ors, rrs;
  u32 pw, px, dummy, rv[2], rdst, wdst, xdst, ra, rso, rso2, tag;
  uint64_t xmask, bad;
};
template <bool ATT, int I, int P> DEVI void dma_w(CS<ATT>& t) {
  using C = Cfg<ATT>;
  dma16<(I & 3) * C::WSLOT + P * 1024>(t.wrs, t.wv[P], I * C::WSTEP, t.wdst);
}
template <bool ATT, int I, int P> DEVI void dma_x(CS<ATT>& t) {
  using C = Cfg<ATT>;
  if constexpr (P + 1 < C::NX) dma16<(I % 3) * C::XSLOT + P * 1024>(t.xrs, t.xv[P], I * 128, t.xdst);
  else dma16m<(I % 3) * C::XSLOT + P * 1024>(t.xrs, t.xv[P], I * 128, t.xdst, t.xmask);
}
template <bool ATT, int I> DEVI void dma_wall(CS<ATT>& t) {
  dma_w<ATT, I, 0>(t); dma_w<ATT, I, 1>(t);
  if constexpr (!ATT) dma_w<ATT, I, 2>(t);
}
template <bool ATT, int I> DEVI void dma_xall(CS<ATT>& t) {
  dma_x<ATT, I, 0>(t); dma_x<ATT, I, 1>(t);
  if constexpr (ATT) dma_x<ATT, I, 2>(t);
}
template <bool ATT> DEVI void rec_wait(CS<ATT>& t) {
  using C = Cfg<ATT>;
  if constexpr (ATT) asm volatile("s_waitcnt vmcnt(0)" : "+v"(t.rec[0]), "+v"(t.rec[1]), "+v"(t.rec[2]), "+v"(t.rec[3]), "+v"(t.rec[4]), "+v"(t.rec[5]), "+v"(t.dummy) :: "memory");
  else asm volatile("s_waitcnt vmcnt(0)" : "+v"(t.rec[0]), "+v"(t.rec[1]), "+v"(t.rec[2]), "+v"(t.rec[3]), "+v"(t.dummy) :: "memory");
}
template <bool ATT> DEVI void rec_test(CS<ATT>& t) {
  rec_wait<ATT>(t);
  u32 bad = 0;
#pragma unroll
  for (int i = 0; i < Cfg<ATT>::NR; ++i) bad |= t.rec[i][1] ^ t.tag;
  t.bad = __builtin_amdgcn_ballot_w64(bad != 0);
}
template <bool ATT> DEVI void rec_retry(CS<ATT>& t) {
#pragma unroll
  for (int i = 0; i < 6; ++i) asm volatile("" : "+v"(t.acc[i][0]), "+v"(t.acc[i][1]), "+v"(t.acc[i][2]), "+v"(t.acc[i][3]));
  while (__builtin_expect(t.bad != 0, 0)) {
    __builtin_amdgcn_s_sleep(2);
    rec_load(t.rec[0], t.srs, t.ro[0]); rec_load(t.rec[1], t.srs, t.ro[1]);
    rec_load(t.rec[2], t.srs, t.ro[2]); rec_load(t.rec[3], t.srs, t.ro[3]);
    if constexpr (ATT) { rec_load(t.rec[4], t.srs, t.ro[4]); rec_load(t.rec[5], t.srs, t.ro[5]); }
    rec_test<ATT>(t);
  }
}
template <bool ATT, int Q> DEVI void piece(CS<ATT>& t) {
  using C = Cfg<ATT>;
  constexpr int ty = C::SC.ty[Q], I = C::SC.idx[Q], P = C::SC.sub[Q];
  if constexpr (ty == T_X) dma_x<ATT, I, P>(t);
  if constexpr (ty == T_W) dma_w<ATT, I, P>(t);
  if constexpr (ty == T_R) dma16<P * 1024>(t.rrs, t.rv[P], 0, t.rdst);
  if constexpr (ty == T_PW) touch(t.dummy, t.wrs, t.pw, P * (C::WSTEP / 32));
  if constexpr (ty == T_PX) touch(t.dummy, t.xrs, t.px, P);
  if constexpr (ty == T_REC) rec_load(t.rec[P], t.srs, t.ro[P]);
}
template <bool ATT, int ST, int Q> DEVI void hook(CS<ATT>& t, AS3 char* sm) {
  using C = Cfg<ATT>;
  constexpr int M = Q >= 14 ? ST + 1 : ST, R = Q >= 14 ? Q - 14 : Q + 10;
  if constexpr (C::at(M, R) >= 0) piece<ATT, C::at(M, R)>(t);
  if constexpr (M == 19 && R == 11) { t.rt[0] = ldtr(sm + t.ra); t.rt[1] = ldtr(sm + t.ra + 256); }
  if constexpr (M == 19 && R == 13) { t.rt[2] = ldtr(sm + t.ra + 8); t.rt[3] = ldtr(sm + t.ra + 264); }
  if constexpr (M == 19 && R == 19) st128(__builtin_bit_cast(u32x4, cat(t.rt[0], t.rt[1])), t.ors, t.rso);
  if constexpr (M == 19 && R == 21) st128(__builtin_bit_cast(u32x4, cat(t.rt[2], t.rt[3])), t.ors, t.rso2);
}
template <bool ATT, int ST, int I> DEVI bf16x8 rd_w(CS<ATT>& t, AS3 char* sm) {
  if constexpr (ATT) {
    constexpr int o = (ST & 3) * 16384;
    return cat(ldtr(sm + t.wa[I] + o), ldtr(sm + t.wa[I] + o + 2048));
  } else {
    constexpr int o = (ST & 1) * 24576 + (I & 1) * 12288 + (I >> 1) * 128;
    return cat(ldtr(sm + t.wa[(ST >> 1) & 1] + o), ldtr(sm + t.wa[(ST >> 1) & 1] + o + 1536));
  }
}
template <bool ATT, int ST, int I> DEVI bf16x8 rd_x(CS<ATT>& t, AS3 char* sm) {
  return ld128(sm + t.xa[ST & 1] + ((ST >> 1) % 3) * Cfg<ATT>::XSLOT + I * 2048);
}
template <bool ATT, int ST, int I> DEVI void rdS(CS<ATT>& t, AS3 char* sm) {
  if constexpr (ST < 20) {
    if constexpr (ATT) t.S[(6 * ST + I) & 3] = rd_x<ATT, ST, I>(t, sm);
    else t.S[(6 * ST + I) & 3] = rd_w<ATT, ST, I>(t, sm);
  }
}
template <bool ATT, int ST, int I> DEVI void rdH(CS<ATT>& t, AS3 char* sm) {
  if constexpr (ST < 20) {
    if constexpr (ATT) t.H[ST & 1][I] = rd_w<ATT, ST, I>(t, sm);
    else t.H[ST & 1][I] = rd_x<ATT, ST, I>(t, sm);
  }
}
template <bool ATT, int ST, int I, int J> DEVI void mm(CS<ATT>& t, AS3 char* sm) {
  sched();
  {
  if constexpr (ATT) t.acc[I][J] = mfma(t.H[ST & 1][J], t.S[(6 * ST + I) & 3], t.acc[I][J]);
  else t.acc[I][J] = mfma(t.S[(6 * ST + I) & 3], t.H[ST & 1][J], t.acc[I][J]);
  }
  sched();
  hook<ATT, ST, I * 4 + J>(t, sm);
  sched();
}
template <bool A, int ST> DEVI void stage(CS<A>& t, AS3 char* sm) {
  mm<A, ST, 0, 0>(t, sm); rdS<A, ST, 3>(t, sm); mm<A, ST, 0, 1>(t, sm); mm<A, ST, 0, 2>(t, sm); mm<A, ST, 0, 3>(t, sm);
  mm<A, ST, 1, 0>(t, sm); rdS<A, ST, 4>(t, sm); mm<A, ST, 1, 1>(t, sm); mm<A, ST, 1, 2>(t, sm); mm<A, ST, 1, 3>(t, sm);
  mm<A, ST, 2, 0>(t, sm); rdS<A, ST, 5>(t, sm); mm<A, ST, 2, 1>(t, sm); mm<A, ST, 2, 2>(t, sm); mm<A, ST, 2, 3>(t, sm);
  mm<A, ST, 3, 0>(t, sm); mm<A, ST, 3, 1>(t, sm);
  if constexpr (ST < 19) { wait_lgkm<0>(); wait_vm<Cfg<A>::vm(ST)>(); bar(); sched(); }
  rdS<A, ST + 1, 0>(t, sm); mm<A, ST, 3, 2>(t, sm); rdH<A, ST + 1, 0>(t, sm); mm<A, ST, 3, 3>(t, sm); rdH<A, ST + 1, 1>(t, sm);
  mm<A, ST, 4, 0>(t, sm); rdS<A, ST + 1, 1>(t, sm); mm<A, ST, 4, 1>(t, sm); rdH<A, ST + 1, 2>(t, sm);
  mm<A, ST, 4, 2>(t, sm); rdH<A, ST + 1, 3>(t, sm); mm<A, ST, 4, 3>(t, sm);
  mm<A, ST, 5, 0>(t, sm); rdS<A, ST + 1, 2>(t, sm); mm<A, ST, 5, 1>(t, sm); mm<A, ST, 5, 2>(t, sm); mm<A, ST, 5, 3>(t, sm);
  if constexpr (ST == 18) { rec_test<A>(t); sched(); }
  if constexpr (ST < 19) stage<A, ST + 1>(t, sm);
}

struct Ptrs { const char *x, *w0, *w1, *wo, *wd; char *a2f, *w2f, *st; };

template <bool ATT> DEVI void k1_main(CS<ATT>& t, int w, int mt, int ns, const Ptrs& P, AS3 char* sm) {
  using C = Cfg<ATT>;
  const int lane = threadIdx.x & 63, wh = w >> 2, wq = w & 3;
  const int unit = blockIdx.x * 8 + w;
  t.xrs = mkrs(P.x, S_ * D_ * 2);
  t.srs = mkrs(P.st, S_ * 8 + 4);
  StatLd sl;
  stat_load(sl, t.xrs, t.srs, unit, lane);
#pragma unroll
  for (int i = 0; i < C::NX; ++i) {
    const int r = (wq * C::NX + i) * 8 + (lane >> 3);
    int tok;
    if (ATT) tok = 2 * min(max(mt * UT_ATT - 7 + r, 0), 1023) + wh;
    else tok = tok_of(min((2 * mt + wh) * HT_MLP + r, S_ - 1));
    t.xv[i] = tok * 1280 + (((lane & 7) ^ ((r >> 1) & 7)) << 4);
  }
  t.xdst = (u32)(size_t)sm + C::XR + w * (C::NX * 1024);
  t.xmask = wq == 3 ? 0x00ffffffffffffffull : ~0ull;
  dma_xall<ATT, 0>(t);
  if (ATT) {
    t.wrs = mkrs(P.w0 + ns * 160, D_ * 3 * D_ * 2 - ns * 160);
#pragma unroll
    for (int i = 0; i < C::NW; ++i) {
      int row = 4 * w + 2 * i + (lane >> 5);
      int rho = (row & 3) + 4 * ((row >> 3) & 1);
      int c = ((lane & 31) - 2 * rho) & 31;
      int so = c < 10 ? 2560 + c * 16 : (c == 10 || c == 31) ? 0 : c <= 20 ? (c - 11) * 16 : 1280 + (c - 21) * 16;
      t.wv[i] = row * 3840 + so;
    }
  } else {
    t.wrs = mkrs((wh ? P.w1 : P.w0) + ns * 384, D_ * F_ * 2 - ns * 384);
#pragma unroll
    for (int i = 0; i < C::NW; ++i) {
      int Q = i * 64 + lane;
      int r = Q / 24, c = Q - r * 24, row = wq * 8 + r;
      int u = (c >> 1) ^ ((row >> 1) & 1) ^ ((row >> 2) & 2);
      t.wv[i] = row * 3456 + u * 32 + (c & 1) * 16;
    }
  }
  t.wdst = (u32)(size_t)sm + w * (C::NW * 1024);
  dma_wall<ATT, 0>(t); dma_wall<ATT, 1>(t);
  sched();
  {
    const int xcd = blockIdx.x & 7, idx = blockIdx.x >> 3, xn = xcd & 3;
    const int id = ATT ? (idx * 8 + w) * 18 + lane : ((idx - 12) * 4 + wq) * 24 + lane;
    const int r = id / 9, l = id - r * 9;
    const int col = ATT ? ((xn * 320) & ~127) - ns * 160 + (l / 3) * (1280 - 384) + l * 128 : (2 * xn - ns) * 384 + l * 128;
    t.pw = (lane < (ATT ? 18 : 24) && r < 192) ? (128 + r) * (C::WSTEP / 32) + col : OOB;
    t.px = (lane < 20 && idx < 30) ? ((xcd >> 2) * 848 + (idx * 8 + w) * 5 + (lane >> 2)) * 1280 + (3 + (lane & 3)) * 128 : OOB;
    t.dummy = 0;
  }
  {
    const int kb = unit / 24, r24 = unit - kb * 24, ni = r24 / 3, part = r24 - ni * 3;
    const bool has = kb < KB2;
    t.rrs = kb < 20 ? mkrs(P.wo, D_ * D_ * 2) : mkrs(P.wd, F_ * D_ * 2);
    const int kl = (kb + 4) % 6, cs = kb < 16 ? 0 : kb < 20 ? 1 : kl < 4 ? 2 : 3;
    const int base = cs == 0 ? 40 * kb : cs == 1 ? 160 * kb - 2544 : cs == 2 ? (kb - 20) * 32 - 16 * kl : (kb - 20) * 32;
    const int c2 = (0x04401404 >> (cs * 8)) & 255, c3 = (0x08042808 >> (cs * 8)) & 255, c4 = (0x10085014 >> (cs * 8)) & 255;
    const int row = base + ((lane >> 2) & 3) + c2 * ((lane >> 4) & 1) + c3 * (lane >> 5);
    t.rv[0] = has ? row * 1280 + ni * 160 + part * 64 + (lane & 3) * 16 : OOB;
    t.rv[1] = t.rv[0] + c4 * 1280;
    t.rdst = (u32)(size_t)sm + C::RT + w * 2048;
    t.ors = mkrso(P.w2f, W2F_BYTES, has ? (ni * KB2 + kb) * 5120 + part * 2048 : 0);
    t.rso = has ? lane * 16 : OOB;
    t.rso2 = has && part < 2 ? lane * 16 + 1024 : OOB;
    t.ra = C::RT + w * 2048 + (8 * (lane >> 4) + ((lane >> 2) & 3)) * 64 + (lane & 3) * (part == 2 ? 8 : 16);
  }
  sched();
  if (wq == 3 && lane < 24) {
    u32x4 ones = {0x3f803f80u, 0x3f803f80u, 0x3f803f80u, 0x3f803f80u};
    *(AS3 u32x4*)(sm + C::XR + (lane >> 3) * C::XSLOT + (w + 1) * (C::NX * 1024) - 128 + (lane & 7) * 16) = ones;
  }
#pragma unroll
  for (int i = 0; i < C::NR; ++i) {
    const int r = 16 * i + (lane & 15);
    int tok;
    if (ATT) tok = 2 * min(max(mt * UT_ATT - 7 + r, 0), 1023) + wh;
    else tok = tok_of(min((2 * mt + wh) * HT_MLP + r, S_ - 1));
    t.ro[i] = tok * 8;
  }
#pragma unroll
  for (int i = 0; i < 6; ++i)
#pragma unroll
    for (int c = 0; c < 4; ++c) t.acc[i][c] = f32x4{0.f, 0.f, 0.f, 0.f};
  t.tag = stat_pub<2 * C::NW + C::NX>(sl, t.srs, unit, lane);
  sched();
  wait_lgkm<0>();
  wait_vm<C::vm(-1)>();
  bar();
  sched();
  rdS<ATT, 0, 0>(t, sm); rdH<ATT, 0, 0>(t, sm); rdH<ATT, 0, 1>(t, sm); rdH<ATT, 0, 2>(t, sm); rdH<ATT, 0, 3>(t, sm);
  rdS<ATT, 0, 1>(t, sm); rdS<ATT, 0, 2>(t, sm);
  stage<ATT, 0>(t, sm);
  rec_retry<ATT>(t);
  sched();
}

DEVI void k1_mlp(int w, int mt, int fs, const Ptrs& P, AS3 char* sm) {
  char* a2f = P.a2f;
  using C = Cfg<false>;
  const int lane = threadIdx.x & 63, wm = w >> 2, wn = w & 3;
  const int G = lane >> 4, tq = lane & 15;
  CS<false> t;
  {
    int q = (lane >> 2) & 3, p = lane & 3;
    t.wa[0] = opq((8 * G + q) * 384 + ((wn ^ (q >> 1) ^ ((G & 1) << 1)) << 5) + 8 * p);
    t.wa[1] = opq(t.wa[0] + 49152);
    t.xa[0] = opq(C::XR + (wm * 64 + tq) * 128 + ((G ^ (tq >> 1)) << 4));
    t.xa[1] = opq(t.xa[0] ^ 64);
  }
  k1_main<false>(t, w, mt, fs, P, sm);
  f32x4 rc[4];
#pragma unroll
  for (int i = 0; i < 4; ++i) {
    const float den = hi16(t.rec[i][0]), rs = __builtin_amdgcn_rcpf(den);
    rc[i] = f32x4{-lo16(t.rec[i][0]), -2.30220820f * rs, -0.10294324f * rs * rs * rs, den * den};
  }
  u32 vo[4];
#pragma unroll
  for (int i = 0; i < 4; ++i) {
    const int r = i * 16 + tq, sg = (2 * mt + wm) * HT_MLP + r;
    vo[i] = (r < HT_MLP && sg < S_) ? a2f_off(sg) : OOB;
  }
  const rsrc_t ors = mkrso(a2f, A2F_BYTES, (20 + fs * 6 + wn) * 4096);
  const rsrc_t ors2 = mkrso(a2f, A2F_BYTES, (24 + fs * 6 + (wn >> 1)) * 4096 + (wn & 1) * 512);
  u32 hp[3][4][2];
#pragma unroll
  for (int b = 0; b < 3; ++b) {
    f32x4 cg, cu;
#pragma unroll
    for (int r = 0; r < 4; ++r) { cg[r] = dppf<0x15F>(t.acc[2 * b][3][r]); cu[r] = dppf<0x15F>(t.acc[2 * b + 1][3][r]); }
#pragma unroll
    for (int i = 0; i < 4; ++i) {
      const f32x2 nm = bc2(rc[i][0]), ca = bc2(rc[i][1]), cb = bc2(rc[i][2]), cd = bc2(rc[i][3]);
#pragma unroll
      for (int e = 0; e < 2; ++e) {
        f32x2 g = fma2(e ? half2<1>(cg) : half2<0>(cg), nm, e ? half2<1>(t.acc[2 * b][i]) : half2<0>(t.acc[2 * b][i]));
        f32x2 u = fma2(e ? half2<1>(cu) : half2<0>(cu), nm, e ? half2<1>(t.acc[2 * b + 1][i]) : half2<0>(t.acc[2 * b + 1][i]));
        f32x2 z = g * fma2(g * g, cb, ca);
        f32x2 ex = {__builtin_amdgcn_exp2f(z[0]), __builtin_amdgcn_exp2f(z[1])};
        f32x2 d = fma2(ex, cd, cd);
        f32x2 rd = {__builtin_amdgcn_rcpf(d[0]), __builtin_amdgcn_rcpf(d[1])};
        hp[b][i][e] = pk2v(g * u * rd);
      }
      if (b == 1) st128(u32x4{hp[0][i][0], hp[0][i][1], hp[1][i][0], hp[1][i][1]}, ors, vo[i] + G * 256);
      if (b == 2 && (i & 1))
        st128(pair_rows(hp[2][i - 1][0], hp[2][i - 1][1], hp[2][i][0], hp[2][i][1]), ors2, ((G & 1) ? vo[i] : vo[i - 1]) + (G >> 1) * 256);
    }
  }
}

DEVI void k1_att(int w, int mt, int hg, const Ptrs& P, AS3 char* sm) {
  char* a2f = P.a2f;
  using C = Cfg<true>;
  const int lane = threadIdx.x & 63, hl = w & 3, par = w >> 2;
  const int G = lane >> 4, tq = lane & 15, q = (lane >> 2) & 3, p = lane & 3;
  CS<true> t;
  {
    int rho = q + 4 * (G & 1);
    int rowb = (8 * G + q) * 512;
    int hb = hl * 40;
    int o0 = 176 + 8 * p, o1 = 336 + 8 * p;
    int o2 = p == 0 ? 176 + 32 : p == 1 ? 0 : p == 2 ? 8 : 24;
    int o3 = p == 0 ? 336 + 32 : p == 1 ? 32 : p == 2 ? 16 : 8;
    t.wa[0] = opq(rowb + ((o0 + hb + 32 * rho) & 511));
    t.wa[1] = opq(rowb + ((o1 + hb + 32 * rho) & 511));
    t.wa[2] = opq(rowb + ((o2 + hb + 32 * rho) & 511));
    t.wa[3] = opq(rowb + ((o3 + hb + 32 * rho) & 511));
    t.xa[0] = opq(C::XR + (par * 96 + tq) * 128 + ((G ^ (tq >> 1)) << 4));
    t.xa[1] = opq(t.xa[0] ^ 64);
  }
  k1_main<true>(t, w, mt, hg, P, sm);
  f32x2 ms[6];
#pragma unroll
  for (int T = 0; T < 6; ++T) ms[T] = f32x2{-lo16(t.rec[T][0]), __builtin_amdgcn_rcpf(hi16(t.rec[T][0]))};
  f32x4 cs[4];
#pragma unroll
  for (int c = 0; c < 4; ++c)
#pragma unroll
    for (int r = 0; r < 4; ++r) cs[c][r] = dppf<0x15F>(t.acc[5][c][r]);
  AS3 char* vs = sm + w * 6144;
  const u32 g0m = G == 0 ? ~0u : 0u;
  u32x4 qf[6], kf[6];
#pragma unroll
  for (int T = 0; T < 6; ++T) {
    const f32x2 nm = bc2(ms[T][0]), rs = bc2(ms[T][1]), r2 = bc2(G == 0 ? 1.f : ms[T][1]);
    u32 c00 = pk2v(fma2(half2<0>(cs[0]), nm, half2<0>(t.acc[T][0]))), c01 = pk2v(fma2(half2<1>(cs[0]), nm, half2<1>(t.acc[T][0])));
    u32 c10 = pk2v(fma2(half2<0>(cs[1]), nm, half2<0>(t.acc[T][1])) * rs), c11 = pk2v(fma2(half2<1>(cs[1]), nm, half2<1>(t.acc[T][1])) * rs);
    u32 c20 = pk2v(fma2(half2<0>(cs[2]), nm, half2<0>(t.acc[T][2])) * r2), c21 = pk2v(fma2(half2<1>(cs[2]), nm, half2<1>(t.acc[T][2])) * r2);
    u32 c30 = pk2v(fma2(half2<0>(cs[3]), nm, half2<0>(t.acc[T][3])) * rs), c31 = pk2v(fma2(half2<1>(cs[3]), nm, half2<1>(t.acc[T][3])) * rs);
    qf[T] = u32x4{c00, c01, c20 & g0m, c21 & g0m};
    kf[T] = u32x4{c10, c11, c30 & g0m, c31 & g0m};
    *(AS3 u32x4*)(vs + (16 * T + tq) * 64 + ((G ^ ((tq >> 1) & 3)) << 4)) = u32x4{c20, c21, c30, c31};
  }
  int va0, va1;
  {
    int row = 4 * G + q;
    int g0 = p == 0 ? 1 : p == 3 ? 3 : 2, h0 = p == 2 ? 8 : 0;
    va0 = row * 64 + ((g0 ^ ((row >> 1) & 3)) << 4) + h0;
    va1 = row * 64 + ((1 ^ ((row >> 1) & 3)) << 4) + 8;
  }
  const float NEG = -__builtin_inff();
  const f32x4 z4 = {0.f, 0.f, 0.f, 0.f};
  bool oka[4], ok0[4];
  f32x4 bb;
#pragma unroll
  for (int r = 0; r < 4; ++r) {
    const int key = 4 * G + r;
    oka[r] = key <= tq && key >= tq - 7;
    ok0[r] = oka[r] && (mt > 0 || key >= 7);
    bb[r] = key >= tq + 9 ? 0.f : NEG;
  }
  const u32 ma0 = (oka[0] ? 0xffffu : 0u) | (oka[1] ? 0xffff0000u : 0u), ma1 = (oka[2] ? 0xffffu : 0u) | (oka[3] ? 0xffff0000u : 0u);
  const u32x4 ones = {0x3f803f80u, 0x3f803f80u, 0x3f803f80u, 0x3f803f80u};
  const float qs = 0.22360679775f * 1.44269504089f;
  const int H = hg * 4 + hl, u0 = mt * UT_ATT - 7;
  const rsrc_t ors = mkrso(a2f, A2F_BYTES, (H >> 1) * 4096 + (H & 1) * 512);
  const rsrc_t ors2 = mkrso(a2f, A2F_BYTES, (16 + (H >> 3)) * 4096 + ((H >> 1) & 3) * 256 + (H & 1) * 8);
  u32x4 pf[6];
#pragma unroll
  for (int T = 0; T < 6; ++T) {
    f32x4 sa = mfmau(kf[T], qf[T], z4), s;
    if (T > 0) {
      f32x4 sb = mfmau(kf[T > 0 ? T - 1 : 0], qf[T], bb);
#pragma unroll
      for (int r = 0; r < 4; ++r) s[r] = oka[r] ? sa[r] : sb[r];
    } else {
#pragma unroll
      for (int r = 0; r < 4; ++r) s[r] = ok0[r] ? sa[r] : NEG;
    }
    const float mx = xg_max(fmaxf(fmaxf(s[0], s[1]), fmaxf(s[2], s[3])));
    const float c = ms[T][1] * qs;
    const f32x2 e0 = fma2(half2<0>(s), bc2(c), bc2(-mx * c)), e1 = fma2(half2<1>(s), bc2(c), bc2(-mx * c));
    const u32 p0 = pk2(__builtin_amdgcn_exp2f(e0[0]), __builtin_amdgcn_exp2f(e0[1]));
    const u32 p1 = pk2(__builtin_amdgcn_exp2f(e1[0]), __builtin_amdgcn_exp2f(e1[1]));
    pf[T] = u32x4{p0 & ma0, p1 & ma1, p0 & ~ma0, p1 & ~ma1};
  }
  s16x4 vp0 = {0, 0, 0, 0}, vp1 = {0, 0, 0, 0};
  u32 oa[2][2], ob[2][2];
#pragma unroll
  for (int T = 0; T < 6; ++T) {
    s16x4 vc0 = ldtr(vs + va0 + T * 1024), vc1 = ldtr(vs + va1 + T * 1024);
    const bf16x8 pb = __builtin_bit_cast(bf16x8, pf[T]);
    f32x4 sm4 = mfma(__builtin_bit_cast(bf16x8, ones), pb, z4);
    f32x4 o0 = mfma(cat(vc0, vp0), pb, z4);
    f32x4 o1 = mfma(cat(vc1, vp1), pb, z4);
    vp0 = vc0; vp1 = vc1;
    const f32x2 inv = bc2(__builtin_amdgcn_rcpf(sm4[0]));
    oa[T & 1][0] = pk2v(half2<0>(o0) * inv); oa[T & 1][1] = pk2v(half2<1>(o0) * inv);
    ob[T & 1][0] = pk2v(half2<0>(o1) * inv); ob[T & 1][1] = pk2v(half2<1>(o1) * inv);
    if (T & 1) {
      const int i = 16 * (T - 1 + (G & 1)) + tq, u = u0 + i;
      const u32 vo = (i >= 7 && i < 7 + UT_ATT && u < 1024) ? a2f_off((u >> 4) * 32 + par * 16 + (u & 15)) : OOB;
      st128(pair_rows(oa[0][0], oa[0][1], oa[1][0], oa[1][1]), ors, vo + (G >> 1) * 256);
      st64((G & 1) ? u32x2{ob[1][0], ob[1][1]} : u32x2{ob[0][0], ob[0][1]}, ors2, G < 2 ? vo : OOB);
    }
  }
}

__global__ __launch_bounds__(512) void k1(const char* __restrict__ x, const char* __restrict__ wqkv,
                                          const char* __restrict__ wo, const char* __restrict__ wg,
                                          const char* __restrict__ wu, const char* __restrict__ wd,
                                          char* __restrict__ a2f, char* __restrict__ w2f, char* __restrict__ st) {
  __shared__ __attribute__((aligned(1024))) char smem[LDS_K1];
  AS3 char* sm = (AS3 char*)smem;
  const int b = blockIdx.x, xcd = b & 7, idx = b >> 3;
  const int xm = xcd >> 2, xn = xcd & 3;
  const int w = __builtin_amdgcn_readfirstlane(threadIdx.x >> 6);
  if (idx < 12) {
    const int mt = xm * 6 + (idx >> 1), hg = xn * 2 + (idx & 1);
    const Ptrs P = {x, wqkv, wqkv, wo, wd, a2f, w2f, st};
    k1_att(w, mt, hg, P, sm);
    return;
  }
  const int j = idx - 12;
  int mt, fs;
  if (xm == 0) {
    const int q = xn * 20 + j;
    mt = q % 9; fs = q / 9;
  } else {
    if (j >= (xn == 3 ? 19 : 18)) {
      StatLd sl;
      stat_load(sl, mkrs(x, S_ * D_ * 2), mkrs(st, S_ * 8 + 4), b * 8 + w, threadIdx.x & 63);
      stat_pub<0>(sl, mkrs(st, S_ * 8 + 4), b * 8 + w, threadIdx.x & 63);
      return;
    }
    const int e = xn * 18 + j;
    mt = e < 72 ? 9 + (e & 7) : 8; fs = e < 72 ? e >> 3 : 8;
  }
  const Ptrs P = {x, wg, wu, wo, wd, a2f, w2f, st};
  k1_mlp(w, mt, fs, P, sm);
}

constexpr int K2P = 6;
struct K2State {
  f32x4 acc[4][5];
  bf16x8 a[K2P][4], wv[K2P][5];
  const char* pa; const char* pw;
};
template <int I> DEVI void k2_load(K2State& t) {
#pragma unroll
  for (int i = 0; i < 4; ++i) t.a[I % K2P][i] = *(const bf16x8*)(t.pa + (size_t)I * 16384 + i * 1024);
#pragma unroll
  for (int i = 0; i < 5; ++i) t.wv[I % K2P][i] = *(const bf16x8*)(t.pw + (size_t)I * 20480 + i * 1024);
}
template <int I> DEVI void k2_mfma(K2State& t) {
#pragma unroll
  for (int n = 0; n < 5; ++n)
#pragma unroll
    for (int i = 0; i < 4; ++i)
      t.acc[i][n] = __builtin_amdgcn_mfma_f32_16x16x32_bf16(t.wv[I % K2P][n], t.a[I % K2P][i], t.acc[i][n], 0, 0, 0);
}
template <int I> DEVI void k2_step(K2State& t, bool extra) {
  k2_mfma<I>(t);
  sched();
  if constexpr (I + K2P < 18) k2_load<I + K2P>(t);
  if constexpr (I + K2P == 18) { if (extra) k2_load<18>(t); }
  sched();
  if constexpr (I < 17) k2_step<I + 1>(t, extra);
}
template <int I> DEVI void k2_pro(K2State& t) {
  k2_load<I>(t);
  if constexpr (I + 1 < K2P) k2_pro<I + 1>(t);
}

__global__ __launch_bounds__(256, 1) void k2(const char* __restrict__ a2f, const char* __restrict__ w2f,
                                             const char* __restrict__ x, char* __restrict__ out, u32* __restrict__ ep) {
  __shared__ __attribute__((aligned(1024))) char smem[4 * 4 * 5 * 1024];
  AS3 char* sm = (AS3 char*)smem;
  const int b = blockIdx.x, xcd = b & 7, idx = b >> 3;
  const int mi = (xcd >> 1) * 8 + (idx >> 2), ni = (xcd & 1) * 4 + (idx & 3);
  const int lane = threadIdx.x & 63, w = __builtin_amdgcn_readfirstlane(threadIdx.x >> 6);
  K2State t;
  t.pa = a2f + (size_t)(mi * KB2 + w) * 4096 + lane * 16;
  t.pw = w2f + (size_t)(ni * KB2 + w) * 5120 + lane * 16;
  k2_pro<0>(t);
  const int tok = tok_of(mi * 64 + w * 16 + (lane & 15)), G = lane >> 4;
  const size_t ro = (size_t)tok * 1280 + ni * 160;
  u32x4 xr0 = *(const u32x4*)(x + ro + G * 16), xr1 = *(const u32x4*)(x + ro + 64 + G * 16);
  u32x2 xr2 = *(const u32x2*)(x + ro + 128 + G * 8);
#pragma unroll
  for (int i = 0; i < 4; ++i)
#pragma unroll
    for (int n = 0; n < 5; ++n) t.acc[i][n] = f32x4{0.f, 0.f, 0.f, 0.f};
  sched();
  const bool extra = w < 2;
  k2_step<0>(t, extra);
  if (extra) k2_mfma<18>(t);
#pragma unroll
  for (int i = 0; i < 4; ++i)
#pragma unroll
    for (int n = 0; n < 5; ++n) *(AS3 f32x4*)(sm + ((i * 4 + w) * 5 + n) * 1024 + lane * 16) = t.acc[i][n];
  __syncthreads();
  f32x4 r[5];
#pragma unroll
  for (int n = 0; n < 5; ++n) {
    r[n] = *(AS3 f32x4*)(sm + ((w * 4 + 0) * 5 + n) * 1024 + lane * 16);
#pragma unroll
    for (int s = 1; s < 4; ++s) r[n] += *(AS3 f32x4*)(sm + ((w * 4 + s) * 5 + n) * 1024 + lane * 16);
  }
  u32x4 o0 = {pk2(r[0][0] + lo16(xr0[0]), r[0][1] + hi16(xr0[0])), pk2(r[0][2] + lo16(xr0[1]), r[0][3] + hi16(xr0[1])),
              pk2(r[1][0] + lo16(xr0[2]), r[1][1] + hi16(xr0[2])), pk2(r[1][2] + lo16(xr0[3]), r[1][3] + hi16(xr0[3]))};
  u32x4 o1 = {pk2(r[2][0] + lo16(xr1[0]), r[2][1] + hi16(xr1[0])), pk2(r[2][2] + lo16(xr1[1]), r[2][3] + hi16(xr1[1])),
              pk2(r[3][0] + lo16(xr1[2]), r[3][1] + hi16(xr1[2])), pk2(r[3][2] + lo16(xr1[3]), r[3][3] + hi16(xr1[3]))};
  u32x2 o2 = {pk2(r[4][0] + lo16(xr2[0]), r[4][1] + hi16(xr2[0])), pk2(r[4][2] + lo16(xr2[1]), r[4][3] + hi16(xr2[1]))};
  const rsrc_t ors = mkrs(out, S_ * D_ * 2);
  __builtin_amdgcn_raw_buffer_store_b128(o0, ors, (u32)ro + G * 16, 0, 17);
  __builtin_amdgcn_raw_buffer_store_b128(o1, ors, (u32)ro + 64 + G * 16, 0, 17);
  __builtin_amdgcn_raw_buffer_store_b64(o2, ors, (u32)ro + 128 + G * 8, 0, 17);
  if (b == 0 && threadIdx.x == 0) *ep += TAGC;
}

extern "C" void kernel_launch(void* const* d_in, const int* in_sizes, int n_in, void* d_out, int out_size,
                              void* d_ws, size_t ws_size, hipStream_t stream) {
  const char* x = (const char*)d_in[0];
  char* a2f = (char*)d_ws;
  char* w2f = a2f + A2F_BYTES;
  char* st = w2f + W2F_BYTES;
  k1<<<256, 512, 0, stream>>>(x, (const char*)d_in[1], (const char*)d_in[2], (const char*)d_in[4],
                              (const char*)d_in[5], (const char*)d_in[6], a2f, w2f, st);
  k2<<<256, 256, 0, stream>>>(a2f, w2f, x, (char*)d_out, (u32*)(st + S_ * 8));
}
```

```cpp
#include <hip/hip_runtime.h>
#include <stdint.h>
#define MALLW
#define MAPL

typedef unsigned short u16;
typedef unsigned int u32;
typedef __attribute__((ext_vector_type(8))) __bf16 bf16x8;
typedef __attribute__((ext_vector_type(2))) __bf16 bf16x2;
typedef __attribute__((ext_vector_type(4))) short s16x4;
typedef __attribute__((ext_vector_type(8))) short s16x8;
typedef __attribute__((ext_vector_type(2))) float f32x2;
typedef __attribute__((ext_vector_type(4))) float f32x4;
typedef __attribute__((ext_vector_type(4))) u32 u32x4;
typedef __attribute__((ext_vector_type(2))) u32 u32x2;
#define AS3 __attribute__((address_space(3)))
#define DEVI __device__ __forceinline__

constexpr int S_ = 2048, D_ = 640, F_ = 1728;
constexpr int KB2 = 74;
constexpr int A2F_BYTES = 32 * KB2 * 4096;
constexpr int W2F_BYTES = 8 * KB2 * 5120;
constexpr int HT_MLP = 61;
constexpr int UT_ATT = 86;
#define ST_AUX 16

template <int N> DEVI void wait_vm() { asm volatile("s_waitcnt vmcnt(%0)" ::"n"(N) : "memory"); }
template <int N> DEVI void wait_lgkm() { asm volatile("s_waitcnt lgkmcnt(%0)" ::"n"(N) : "memory"); }
DEVI void bar() { __builtin_amdgcn_s_barrier(); }
DEVI void sched() { __builtin_amdgcn_sched_barrier(0); }
DEVI u32 opq(u32 v) { asm volatile("" : "+v"(v)); return v; }

DEVI u32 pk2(float a, float b) {
  f32x2 v = {a, b};
  bf16x2 r = __builtin_convertvector(v, bf16x2);
  return __builtin_bit_cast(u32, r);
}
DEVI u32 pk2v(f32x2 v) { return __builtin_bit_cast(u32, __builtin_convertvector(v, bf16x2)); }
DEVI float rbf(float a) { return __uint_as_float(pk2(a, 0.f) << 16); }
DEVI float lo16(u32 v) { return __uint_as_float(v << 16); }
DEVI float hi16(u32 v) { return __uint_as_float(v & 0xffff0000u); }
DEVI f32x2 bc2(float a) { return f32x2{a, a}; }
DEVI f32x2 fma2(f32x2 a, f32x2 b, f32x2 c) { return __builtin_elementwise_fma(a, b, c); }
template <int I> DEVI f32x2 half2(f32x4 v) { return __builtin_shufflevector(v, v, 2 * I, 2 * I + 1); }

#define NTM 0
#define NTS(bit) ((NTM >> bit) & 1)
#define SPN 3
#define SPW 0
#define NTWL 99
#define NTXL 99
typedef __amdgpu_buffer_rsrc_t rsrc_t;
DEVI rsrc_t mkrs(const char* p, int bytes) { return __builtin_amdgcn_make_buffer_rsrc((void*)p, (short)0, bytes, 0x00020000); }
template <int LOFF, bool NT> DEVI void dma16(rsrc_t rs, u32 voff, u32 soff, u32 lds) {
  if constexpr (NT)
    asm volatile("s_add_u32 m0, %0, %4\n\ts_nop 0\n\tbuffer_load_dwordx4 %1, %2, %3 offen nt lds"
                 :: "s"(lds), "v"(voff), "s"(rs), "s"(soff), "i"(LOFF) : "memory", "scc");
  else
    asm volatile("s_add_u32 m0, %0, %4\n\ts_nop 0\n\tbuffer_load_dwordx4 %1, %2, %3 offen lds"
                 :: "s"(lds), "v"(voff), "s"(rs), "s"(soff), "i"(LOFF) : "memory", "scc");
}
template <int LOFF> DEVI void dma16c(rsrc_t rs, u32 voff, u32 soff, u32 lds) {
  asm volatile("s_add_u32 m0, %0, %4\n\ts_nop 0\n\tbuffer_load_dwordx4 %1, %2, %3 offen sc1 lds"
               :: "s"(lds), "v"(voff), "s"(rs), "s"(soff), "i"(LOFF) : "memory", "scc");
}
template <int LOFF, bool NT> DEVI void dma16m(rsrc_t rs, u32 voff, u32 soff, u32 lds, uint64_t mask) {
  if constexpr (NT)
    asm volatile("s_add_u32 m0, %0, %4\n\ts_mov_b64 exec, %5\n\tbuffer_load_dwordx4 %1, %2, %3 offen nt lds\n\ts_mov_b64 exec, -1"
                 :: "s"(lds), "v"(voff), "s"(rs), "s"(soff), "i"(LOFF), "s"(mask) : "memory", "scc");
  else
    asm volatile("s_add_u32 m0, %0, %4\n\ts_mov_b64 exec, %5\n\tbuffer_load_dwordx4 %1, %2, %3 offen lds\n\ts_mov_b64 exec, -1"
                 :: "s"(lds), "v"(voff), "s"(rs), "s"(soff), "i"(LOFF), "s"(mask) : "memory", "scc");
}
DEVI void st128(u32x4 v, rsrc_t rs, u32 voff) { __builtin_amdgcn_raw_buffer_store_b128(v, rs, voff, 0, ST_AUX); }
DEVI void st64(u32x2 v, rsrc_t rs, u32 voff) { __builtin_amdgcn_raw_buffer_store_b64(v, rs, voff, 0, ST_AUX); }
DEVI void st32(u32 v, rsrc_t rs, u32 voff) { __builtin_amdgcn_raw_buffer_store_b32(v, rs, voff, 0, 16); }
DEVI rsrc_t mkrso(const char* p, int bytes, int off) { return mkrs(p + off, bytes - off); }
DEVI bf16x8 ld128(AS3 char* p) { return *(AS3 bf16x8*)p; }
DEVI s16x4 ldtr(AS3 char* p) { return __builtin_amdgcn_ds_read_tr16_b64_v4i16((AS3 s16x4*)p); }
DEVI bf16x8 cat(s16x4 a, s16x4 b) {
  s16x8 r = __builtin_shufflevector(a, b, 0, 1, 2, 3, 4, 5, 6, 7);
  return __builtin_bit_cast(bf16x8, r);
}
DEVI f32x4 mfma(bf16x8 a, bf16x8 b, f32x4 c) { return __builtin_amdgcn_mfma_f32_16x16x32_bf16(a, b, c, 0, 0, 0); }
DEVI f32x4 mfmau(u32x4 a, u32x4 b, f32x4 c) { return mfma(__builtin_bit_cast(bf16x8, a), __builtin_bit_cast(bf16x8, b), c); }
template <int CTRL> DEVI float dppf(float v) {
  return __int_as_float(__builtin_amdgcn_update_dpp(0, __float_as_int(v), CTRL, 0xf, 0xf, true));
}
DEVI float sum8(float v) {
  v += dppf<0xB1>(v);
  v += dppf<0x4E>(v);
  v += dppf<0x141>(v);
  return v;
}
DEVI float xg_max(float x) {
  auto s = __builtin_amdgcn_permlane16_swap(__float_as_uint(x), __float_as_uint(x), false, false);
  x = __builtin_elementwise_maximum(__uint_as_float(s[0]), __uint_as_float(s[1]));
  auto t = __builtin_amdgcn_permlane32_swap(__float_as_uint(x), __float_as_uint(x), false, false);
  return __builtin_elementwise_maximum(__uint_as_float(t[0]), __uint_as_float(t[1]));
}
DEVI u32x4 pair_rows(u32 a0, u32 a1, u32 b0, u32 b1) {
  auto s = __builtin_amdgcn_permlane16_swap(a0, b0, false, false);
  auto t = __builtin_amdgcn_permlane16_swap(a1, b1, false, false);
  return u32x4{s[0], t[0], s[1], t[1]};
}
DEVI int tok_of(int sg) { return (sg & ~31) | ((sg & 15) << 1) | ((sg >> 4) & 1); }
DEVI u32 a2f_off(int sg) { return (sg >> 6) * (KB2 * 4096) + ((sg >> 4) & 3) * 1024 + (sg & 15) * 16; }
constexpr u32 OOB = 0x80000000u;
#define NTW 1
DEVI int mi_of(int b) { return ((b & 7) >> 1) * 8 + ((b >> 3) & 7); }
DEVI int ni_of(int b) { return (b & 1) * 4 + (((b >> 3) + (b >> 6)) & 3); }
#define WR 5
#define ROTT 0x23004000
#define ROTI 0x33221100
DEVI u32 grp_tab() { return (u32)(0x876543876543ull >> (((ROTT >> ((blockIdx.x >> 5) * 4)) & 7) * 4)); }
DEVI u32 grp_of(u32 gt, int s) { return s < 6 ? (gt >> (4 * s)) & 15 : s == 6 ? 2 : s - 7; }

#define CAP 5
#define PLO 9
#define PHI 22
#define PTL 15
#define RINT 3
#define PFLW -1,0,4
#define PFLX 99,0,1
constexpr int pfl_w[] = {PFLW}, pfl_x[] = {PFLX};
#define PRE 2
#define PRO2 0
#define SINT 0
#define CODEB 40960
#define M0LO 10
#define CAP0 CAP
#define WPA 13
#define RTI 99
#define RECA 13
#define WPOLL 14
#define WE 3
#define WR0 16
#define WR1 20
#define NWT 0
#define E0 10
#define RD 3
#define RGAP 0
#define RS 1
#define EARLY2 0
#define FCA 14
#define FCM 15
#define VLT 2
#define WFS 1
#define TWN 7
#define TWI 99
#define RECN 2
#define RECI 13
#define VC0 5
enum { T_TW, T_X, T_W, T_R, T_S, T_REC, T_PW, T_PX, T_RS, T_RF, T_WF, T_V, T_WR, T_SL, T_RT };
struct Sched { int n, ty[160], idx[160], sub[160], first[23], vm[22]; };
constexpr int chunk_free(int NW, int NX, int c) {
  if (NW == 2 && c >= VC0 && c < VC0 + 6) return 99;
  if (c < 4 * NW) return c < 2 * NW ? 17 + c / NW : NW == 2 ? 19 : 20;
  if (NW == 2 && c > 16 - VLT && c <= 16) return 19;
  if (c < 4 * NW + 3 * NX) { const int x = (c - 4 * NW) / NX; return x == 0 ? (NW == 2 ? 20 : 19) : x == 1 ? 16 : 18; }
  return NW == 2 ? (FCA == 15 ? 16 : FCA == 14 || c > 17 ? 15 : FCA + 1) : (FCM == 15 ? 16 : 15);
}
constexpr void pf_add(Sched& s, int* due, int m) {
  for (int i = 0; i < (int)(sizeof(pfl_w) / 12); ++i)
    if (pfl_w[3 * i] == m) { s.ty[s.n] = T_PW; s.idx[s.n] = pfl_w[3 * i + 1]; s.sub[s.n] = pfl_w[3 * i + 2]; due[s.n] = 99; ++s.n; }
  for (int i = 0; i < (int)(sizeof(pfl_x) / 12); ++i)
    if (pfl_x[3 * i] == m) { s.ty[s.n] = T_PX; s.idx[s.n] = pfl_x[3 * i + 1]; s.sub[s.n] = pfl_x[3 * i + 2]; due[s.n] = 99; ++s.n; }
}
constexpr Sched mk_sched(int NW, int NX, int NR) {
  Sched s = {};
  int al[128] = {}, du[128] = {}, ty[128] = {}, ix[128] = {}, sb[128] = {}, n = 0;
  for (int j = 0; j < 10; ++j) {
    for (int p = 0; p < NX; ++p) { ty[n] = T_X; ix[n] = j; sb[n] = p; al[n] = j < 3 ? -1 : 2 * j - 4; du[n] = 2 * j - 1; ++n; }
    for (int i = 2 * j; i < 2 * j + 2; ++i)
      for (int p = 0; p < NW; ++p) { ty[n] = T_W; ix[n] = i; sb[n] = p; al[n] = i < 4 ? -1 : i - 3; du[n] = i < 19 ? i - 1 : 17; ++n; }
  }
  int due[160] = {}, q = 0;
  for (int m = -1; m <= 20; ++m) {
    s.first[m + 1] = s.n;
    int c = 0;
    if (m < 0) {
      for (; q < NX + PRE * NW; ++q) { s.ty[s.n] = ty[q]; s.idx[s.n] = ix[q]; s.sub[s.n] = sb[q]; due[s.n] = du[q]; ++s.n; }
      for (int p = 0; p < 3; ++p) { s.ty[s.n] = T_SL; s.sub[s.n] = p; due[s.n] = 99; ++s.n; }
      pf_add(s, due, m);
    }
    if (NW == 3 && m == RTI) { s.ty[s.n] = T_RT; due[s.n] = 99; ++s.n; }
    if (m == TWI) { s.ty[s.n] = T_TW; due[s.n] = 99; ++s.n; }
    if (m == (NW == 2 ? WPA : WPOLL)) { s.ty[s.n] = T_WF; due[s.n] = NW == 2 ? FCA : FCM; ++s.n; }
    if (NW == 3 && m == RINT + RD + RS + 3) { s.ty[s.n] = T_RF; due[s.n] = 99; ++s.n; }
    if (NW == 3 && m == RINT + RD + 2) for (int p = 0; p < 2; ++p) { s.ty[s.n] = T_RS; s.sub[s.n] = p; due[s.n] = RINT + RD + RS + 2; ++s.n; }
    while (q < n && c < (m < 0 ? PRO2 : m == 0 ? CAP0 : CAP) && al[q] <= m) {
      s.ty[s.n] = ty[q]; s.idx[s.n] = ix[q]; s.sub[s.n] = sb[q]; due[s.n] = du[q]; ++s.n; ++q; ++c;
    }
    if (m >= 0) pf_add(s, due, m);
    if (m == SINT) { s.ty[s.n] = T_S; s.sub[s.n] = s.n - (NX + PRE * NW + 3); due[s.n] = 99; ++s.n; }
    for (int p = 0; p < 2; ++p) if (NW == 3 && m == RINT + p * RGAP) { s.ty[s.n] = T_R; s.sub[s.n] = p; due[s.n] = RINT + RD; ++s.n; }
    for (int p = 0; p < NR; ++p) if (m == (NW == 2 ? RECA : RECI) - 2 * (p / RECN)) { s.ty[s.n] = T_REC; s.sub[s.n] = p; due[s.n] = 16; ++s.n; }
    for (int p = 0; p < 20; ++p) if (chunk_free(NW, NX, p) == m) { s.ty[s.n] = T_V; s.sub[s.n] = p; due[s.n] = 99; ++s.n; }
    for (int p = 0; p < NWT; ++p) if (WR0 + p * (WR1 - WR0 + 1) / (NWT ? NWT : 1) == m) { s.ty[s.n] = T_WR; s.sub[s.n] = p; due[s.n] = 99; ++s.n; }
    int need = 0;
    for (int k = 0; k < s.n; ++k) if (due[k] <= m) need = k + 1;
    s.vm[m + 1] = s.n - need;
  }
  s.first[22] = s.n;
  return s;
}
template <bool ATT> struct Cfg {
  static constexpr int NW = ATT ? 2 : 3, NX = ATT ? 3 : 2;
  static constexpr int NR = ATT ? 6 : 4;
  static constexpr int WSLOT = NW * 8192, XSLOT = NX * 8192;
  static constexpr int XR = 4 * WSLOT, RT = XR + 3 * XSLOT;
  static constexpr int WSTEP = 32 * (ATT ? 3 * D_ * 2 : F_ * 2);
  static constexpr Sched SC = mk_sched(NW, NX, NR);
  static constexpr int vm(int m) { return SC.vm[m + 1]; }
  static constexpr int at(int m, int r) {
    const int f = SC.first[m + 1], c = SC.first[m + 2] - f;
    const int lo = m == 0 ? M0LO : c > 4 || m == 20 || (ATT && m == WPA && WPA == FCA) ? 1 : PLO, h0 = m == 20 ? 9 : m >= PTL ? 22 : PHI, hi = h0 < lo + c - 1 ? lo + c - 1 : h0;
    for (int k = 0; k < c; ++k) if (lo + (c > 1 ? k * (hi - lo) / (c - 1) : 0) == r) return f + k;
    return -1;
  }
};
template <bool A> constexpr bool sched_ok() {
  for (int m = 0; m <= 20; ++m) {
    int c = 0;
    for (int r = 0; r < 24; ++r) c += Cfg<A>::at(m, r) >= 0;
    if (c != Cfg<A>::SC.first[m + 2] - Cfg<A>::SC.first[m + 1] || Cfg<A>::vm(m) > 63) return false;
  }
  return true;
}
static_assert(sched_ok<false>() && sched_ok<true>(), "schedule");
constexpr int LDS_K1 = 163840;
constexpr u32 TAGC = 0x9E3779B9u;
#define FS 64
constexpr int WF_OFF = S_ * 8 + 256, AF_OFF = WF_OFF + 256 * 64, ST_BYTES = AF_OFF + 32 * 4096;
constexpr int SPIN_CAP = 1 << 17;

DEVI int stat_tok(int b, int w) { return (b & 7) * 256 + (b >> 3) * 8 + w; }
struct StatLd { u32x4 a; u32 b, e; };
DEVI void stat_load(StatLd& s, rsrc_t xrs, rsrc_t srs, int tok, int lane) {
#define SNT ""
  asm volatile("buffer_load_dwordx4 %0, %3, %5, %6 offen" SNT "\n\tbuffer_load_dword %1, %4, %5, %6 offen offset:1024" SNT "\n\t"
               "buffer_load_dword %2, off, %7, %8"
               : "=&v"(s.a), "=&v"(s.b), "=&v"(s.e)
               : "v"(lane * 16), "v"(lane * 4), "s"(xrs), "s"(tok * 1280), "s"(srs), "s"(S_ * 8) : "memory");
}
DEVI float wsum(float v) {
  v = sum8(v);
  v += dppf<0x140>(v);
  auto s = __builtin_amdgcn_permlane16_swap(__float_as_uint(v), __float_as_uint(v), false, false);
  v = __uint_as_float(s[0]) + __uint_as_float(s[1]);
  auto t = __builtin_amdgcn_permlane32_swap(__float_as_uint(v), __float_as_uint(v), false, false);
  return __uint_as_float(t[0]) + __uint_as_float(t[1]);
}
template <int N> DEVI u32 stat_pub(StatLd& s, rsrc_t srs, int tok, int lane) {
  asm volatile("s_waitcnt vmcnt(%3)" : "+v"(s.a), "+v"(s.b), "+v"(s.e) : "n"(N) : "memory");
  const u32 tag = s.e + TAGC;
  f32x2 e[5];
#pragma unroll
  for (int i = 0; i < 4; ++i) e[i] = f32x2{lo16(s.a[i]), hi16(s.a[i])};
  e[4] = f32x2{lo16(s.b), hi16(s.b)};
  f32x2 a = (e[0] + e[1]) + (e[2] + e[3]) + e[4];
  const float mean = wsum(a[0] + a[1]) * (1.f / 640.f);
  f32x2 q = bc2(0.f);
#pragma unroll
  for (int i = 0; i < 5; ++i) { const f32x2 d = e[i] - bc2(mean); q = fma2(d, d, q); }
  const float var = wsum(q[0] + q[1]) * (1.f / 640.f);
  const float den = __builtin_amdgcn_sqrtf(rbf(rbf(var) + 1e-5f));
  st64(u32x2{pk2(mean, den), tag}, srs, lane == 0 ? tok * 8 : OOB);
  return tag;
}
DEVI void rec_load(u32x2& r, rsrc_t srs, u32 voff) {
  asm volatile("buffer_load_dwordx2 %0, %1, %2, 0 offen sc1" : "=v"(r) : "v"(voff), "s"(srs) : "memory");
}

template <int NL, bool NT> DEVI void touch(u32& d, rsrc_t rs, u32 voff, u32 soff) {
  if constexpr (NT)
    asm volatile("s_bfm_b64 exec, %4, 0\n\tbuffer_load_dword %0, %1, %2, %3 offen nt\n\ts_mov_b64 exec, -1"
                 : "+v"(d) : "v"(voff), "s"(rs), "s"(soff), "n"(NL) : "memory");
  else
    asm volatile("s_bfm_b64 exec, %4, 0\n\tbuffer_load_dword %0, %1, %2, %3 offen\n\ts_mov_b64 exec, -1"
                 : "+v"(d) : "v"(voff), "s"(rs), "s"(soff), "n"(NL) : "memory");
}

#define NE 10
struct WPre { u32x4 v[30]; };
DEVI void wp_ld(WPre& wp, int e0, int e1, rsrc_t wr, u32 l16, u32 wo) {
  const u32 gt = grp_tab();
#pragma unroll
  for (int e = e0; e < e1; ++e) {
    const int n = e % 5;
    wp.v[e] = __builtin_amdgcn_raw_buffer_load_b128(wr, l16 + (n & 3) * 1024, (e < 5 ? wo : grp_of(gt, e / 5 - 1) * 40960) + (n >> 2) * 4096, 16);
  }
}
DEVI u32 wo_of(int w) { return __builtin_amdgcn_readfirstlane((72 + (w & 1) - w) * 5120); }
DEVI void touchc(u32& d, rsrc_t rs, u32 voff, u32 soff) {
  asm volatile("buffer_load_dword %0, %1, %2, %3 offen sc1" : "+v"(d) : "v"(voff), "s"(rs), "s"(soff) : "memory");
}
template <bool ATT> struct CS {
  WPre wp;
  StatLd sl;
  int stok, w, mt, pm;
  const char *wo, *wd;
  char* w2f;
  f32x4 acc[6][4];
  bf16x8 S[4], H[2][4];
  u32 wa[4], xa[2];
  u32 wv[Cfg<ATT>::NW], xv[Cfg<ATT>::NX], ro[Cfg<ATT>::NR];
  u32x2 rec[Cfg<ATT>::NR];
  s16x4 rt[4];
  rsrc_t wrs, xrs, srs, ors, rrs, vrs, prs, trs;
  u32 pw, px, dummy, rv[2], rdst, wdst, xdst, vdst, ra, rso, rso2, tag, wf, wfo, rfo, l16, tw;
  uint64_t xmask, bad;
  float rc[Cfg<ATT>::NR][4], qc[3];
  f32x2 rp[Cfg<ATT>::NR][2];
  f32x4 ba, b0, bb;
  u32 vsa, g0m, vo[6], va[5], ma[4];
};
template <bool ATT, int I, int P> DEVI void dma_w(CS<ATT>& t) {
  using C = Cfg<ATT>;
  dma16<(I & 3) * C::WSLOT + P * 1024, NTS(0) && I < NTWL>(t.wrs, t.wv[P], I * C::WSTEP, t.wdst);
}
template <bool ATT, int I, int P> DEVI void dma_x(CS<ATT>& t) {
  using C = Cfg<ATT>;
  if constexpr (P + 1 < C::NX) dma16<(I % 3) * C::XSLOT + P * 1024, NTS(1) && I < NTXL>(t.xrs, t.xv[P], I * 128, t.xdst);
  else dma16m<(I % 3) * C::XSLOT + P * 1024, NTS(1) && I < NTXL>(t.xrs, t.xv[P], I * 128, t.xdst, t.xmask);
}
template <bool ATT, int I> DEVI void dma_wall(CS<ATT>& t) {
  dma_w<ATT, I, 0>(t); dma_w<ATT, I, 1>(t);
  if constexpr (!ATT) dma_w<ATT, I, 2>(t);
}
template <bool ATT, int I> DEVI void dma_xall(CS<ATT>& t) {
  dma_x<ATT, I, 0>(t); dma_x<ATT, I, 1>(t);
  if constexpr (ATT) dma_x<ATT, I, 2>(t);
}
template <bool ATT, bool WAIT> DEVI void rec_test(CS<ATT>& t) {
  if constexpr (WAIT) wait_vm<0>();
  if constexpr (ATT) asm volatile("" : "+v"(t.rec[0]), "+v"(t.rec[1]), "+v"(t.rec[2]), "+v"(t.rec[3]), "+v"(t.rec[4]), "+v"(t.rec[5]), "+v"(t.dummy) :: "memory");
  else asm volatile("" : "+v"(t.rec[0]), "+v"(t.rec[1]), "+v"(t.rec[2]), "+v"(t.rec[3]), "+v"(t.dummy) :: "memory");
  u32 bad = 0;
#pragma unroll
  for (int i = 0; i < Cfg<ATT>::NR; ++i) bad |= t.rec[i][1] ^ t.tag;
  t.bad = __builtin_amdgcn_ballot_w64(bad != 0);
}
template <bool ATT> DEVI void rec_retry(CS<ATT>& t) {
#pragma unroll
  for (int i = 0; i < 6; ++i) asm volatile("" : "+v"(t.acc[i][0]), "+v"(t.acc[i][1]), "+v"(t.acc[i][2]), "+v"(t.acc[i][3]));
  for (int it = 0; __builtin_expect(t.bad != 0, 0) && it < SPIN_CAP; ++it) {
    __builtin_amdgcn_s_sleep(2);
    rec_load(t.rec[0], t.srs, t.ro[0]); rec_load(t.rec[1], t.srs, t.ro[1]);
    rec_load(t.rec[2], t.srs, t.ro[2]); rec_load(t.rec[3], t.srs, t.ro[3]);
    if constexpr (ATT) { rec_load(t.rec[4], t.srs, t.ro[4]); rec_load(t.rec[5], t.srs, t.ro[5]); }
    rec_test<ATT, true>(t);
  }
}
DEVI void flag_load(u32& r, rsrc_t srs, u32 voff) {
  asm volatile("buffer_load_dword %0, %1, %2, 0 offen sc1" : "=v"(r) : "v"(voff), "s"(srs) : "memory");
}
DEVI void flag_spin(u32 v, u32 tag, rsrc_t srs, u32 voff) {
  u32 cnt;
  asm volatile(
      "s_mov_b32 %1, %5\n"
      "1:\n\t"
      "v_cmp_ne_u32 vcc, %0, %2\n\t"
      "s_cbranch_vccz 2f\n\t"
      "s_sleep 1\n\t"
      "buffer_load_dword %0, %3, %4, 0 offen sc1\n\t"
      "s_sub_u32 %1, %1, 1\n\t"
      "s_waitcnt vmcnt(0)\n\t"
      "s_cbranch_scc0 1b\n"
      "2:"
      : "+v"(v), "=&s"(cnt) : "v"(tag), "v"(voff), "s"(srs), "i"(SPIN_CAP) : "memory", "scc", "vcc");
}
DEVI u32 v_soff(int p) { return grp_of(grp_tab(), WR + p / 5) * 40960 + (p % 5) * 1024; }
template <bool ATT, int Q> DEVI void piece(CS<ATT>& t) {
  using C = Cfg<ATT>;
  constexpr int ty = C::SC.ty[Q], I = C::SC.idx[Q], P = C::SC.sub[Q];
  if constexpr (ty == T_X) dma_x<ATT, I, P>(t);
  if constexpr (ty == T_W) dma_w<ATT, I, P>(t);
  if constexpr (ty == T_R) dma16<P * 1024, NTS(2)>(t.rrs, t.rv[P], 0, t.rdst);
  if constexpr (ty == T_PW) touch<P * (ATT ? 6 : 8), NTS(3)>(t.dummy, t.prs, t.pw, I * 2 * C::WSTEP);
  if constexpr (ty == T_RT) touch<1, false>(t.dummy, t.rrs, t.rv[0], 0);
  if constexpr (ty == T_REC) rec_load(t.rec[P], t.srs, t.ro[P]);
  if constexpr (ty == T_RS) st128(__builtin_bit_cast(u32x4, cat(t.rt[2 * P], t.rt[2 * P + 1])), t.ors, P ? t.rso2 : t.rso);
  if constexpr (ty == T_RF) st32(t.tag, t.srs, t.rfo);
  if constexpr (ty == T_WF) flag_load(t.wf, t.srs, t.wfo);
  if constexpr (ty == T_V) dma16c<P * 8192>(t.vrs, t.l16, v_soff(P), t.vdst);
  if constexpr (ty == T_SL && P == 0) stat_load(t.sl, t.xrs, t.srs, t.stok, threadIdx.x & 63);
  if constexpr (ty == T_S) t.tag = stat_pub<P>(t.sl, t.srs, t.stok, threadIdx.x & 63);
}
template <bool ATT, int Q, int E> DEVI void pro_pieces(CS<ATT>& t) {
  if constexpr (Q < E) { piece<ATT, Q>(t); pro_pieces<ATT, Q + 1, E>(t); }
}
#define SPO 3
constexpr int KBM = KB2 - 28, NPM = KBM / 2;
struct RtK { int base, c2, c3, c4; };
DEVI RtK rt_rows(int kb) {
  const int kl = (kb + 4) % 6, cs = ((kb >= 16) + (kb >= 20) + ((kb >= 20) & (kl >= 4))) * 8;
  return RtK{kb * (int)((0x2020A028u >> cs) & 255) - 16 * (int)(((0x28289F00u >> cs) & 255) + (cs == 16) * kl),
             (0x04401404 >> cs) & 255, (0x08042808 >> cs) & 255, (0x10085014 >> cs) & 255};
}
DEVI u32 wf_off(int ni, int i) {
  const int c0 = 5 * ni >> 3, c1 = (5 * ni + 4) >> 3;
  return WF_OFF + min(i + (c0 * NPM & (6 - i) >> 31), c1 * NPM + NPM + 6) * 64;
}
#define LS0 1
#define LPI 3
#define LPRO 18
#define ESA LPRO
#define ESM LPRO
#define EPA LPRO
#define EPM LPRO
template <bool ATT, int N> DEVI void late(CS<ATT>& t, AS3 char* sm) {
  using C = Cfg<ATT>;
  const int lane = N < LPRO ? threadIdx.x & 63 : opq(t.l16) >> 4, w = t.w, wh = w >> 2, wq = w & 3, mt = t.mt, G = lane >> 4, tq = lane & 15;
  constexpr int R0 = ATT ? 1 : 4;
  if constexpr (!ATT && N < 3) {
    const int cw = t.pm / NPM, kb = (t.pm - cw * NPM) * 2 + wh;
    const u32 oob = (u32)(cw >= 5) << 31;
    if constexpr (N == 0) {
      const RtK k = rt_rows(kb);
      t.rrs = mkrs(kb < 20 ? t.wo : t.wd, (D_ + (kb >= 20) * (F_ - D_)) * D_ * 2);
      t.rv[0] = opq(((k.base + k.c3 * (wq & 1) + k.c4 * (wq >> 1) + G) * 1280 + cw * 256 + tq * 16) | oob);
      t.rv[1] = opq(t.rv[0] + k.c2 * 1280);
      t.rdst = (u32)(size_t)sm + C::RT + w * 2048;
      asm volatile("" :: "s"(t.rrs), "s"(t.rdst));
    }
    if constexpr (N == 1) {
      const int ci = cw * 16 + (lane >> 2), ni = ci / 10, oc = ci - ni * 10, hi = oc >> 3;
      t.rso = opq((ni * (KB2 * 5120) + ((oc >> 2 ^ (kb & ~hi & 1)) << 11) + ((oc & 3) << (6 + hi)) + (lane & 3) * 16) | oob);
      t.rso2 = opq(t.rso + (1024 >> 4 * hi));
    }
    if constexpr (N == 2) {
      t.ors = mkrso(t.w2f, W2F_BYTES, kb * 5120 + wq * 256);
      t.ra = opq(C::RT + w * 2048 + ((lane >> 2) & 3) * 256 + G * 64 + (lane & 3) * 16);
      t.rfo = opq((WF_OFF + (7 + t.pm) * 64) | (u32)(-(lane | w)) & OOB);
      asm volatile("" :: "s"(t.ors));
    }
  }
  if constexpr (N == R0 - 1) {
    const int n2 = ni_of(blockIdx.x);
    t.wfo = opq(wf_off(n2, w + 8 * min(lane, 6)));
    t.vrs = mkrso(t.w2f, W2F_BYTES, (n2 * KB2 + w) * 5120);
    t.vdst = (u32)(size_t)sm + w * 1024;
    asm volatile("" :: "s"(t.vrs), "s"(t.vdst));
  }
  if constexpr (N >= R0 && N < R0 + C::NR) {
    const int r = 16 * (N - R0) + tq;
    t.ro[N - R0] = opq(8 * (ATT ? 2 * min(max(mt * UT_ATT - 7 + r, 0), 1023) + wh : tok_of(min((2 * mt + wh) * HT_MLP + r, S_ - 1))));
  }
  if constexpr (ATT && N == 7) {
    t.vsa = opq(VC0 * 8192 + w * 1024 + tq * 32 + G * 8);
    t.g0m = opq(lane < 16 ? ~0u : 0u);
    const int p = lane & 3, vr = VC0 * 8192 + w * 1024 + (lane >> 2) * 32;
#pragma unroll
    for (int i = 0; i < 4; ++i) t.va[i] = opq(vr + (p == i ? 520 : 536));
    t.va[4] = opq(vr + ((0x03420201 >> 8 * p) & 255) * 8);
  }
  if constexpr (N >= 8 && N < 14) {
    constexpr int i = N - 8;
    if constexpr (ATT) {
      const int r = (i < 3 ? i : i == 3 ? 5 : i == 4 ? G : 4 + G) * 16 + tq, u = mt * UT_ATT - 7 + r;
      t.vo[i] = opq((a2f_off((u >> 4) * 32 + wh * 16 + (u & 15)) + (i < 4 ? (G >> 1) * 256 + (G & 1) * 8 : 0)) |
                    (u32)((r - 7) | (6 + UT_ATT - r) | (1023 - u)) & OOB);
    } else {
      const int r = (i < 4 ? i : (i - 4) * 2 + (G & 1)) * 16 + tq, sg = (2 * mt + wh) * HT_MLP + r;
      t.vo[i] = opq((a2f_off(sg) + (i < 4 ? G : G >> 1) * 256) | (u32)((HT_MLP - 1 - r) | (S_ - 1 - sg)) & OOB);
    }
  }
  if constexpr (ATT && N >= 14 && N < 18) {
    constexpr int r = N - 14;
    const int key = 4 * G + r;
    const bool ca = (key <= tq) & (key >= tq - 7), cb = key >= tq + 9;
    float x0 = ca ? 0.f : -__builtin_inff(), x1 = ca & ((mt > 0) | (key >= 7) | (tq < 7)) ? 0.f : -__builtin_inff(), x2 = cb ? 0.f : -__builtin_inff();
    asm volatile("" : "+v"(x0), "+v"(x1), "+v"(x2));
    t.ba[r] = x0; t.b0[r] = x1; t.bb[r] = x2;
    t.ma[r >> 1] = opq((r & 1 ? t.ma[r >> 1] : 0u) | (ca ? 0xffffu << (r & 1) * 16 : 0u));
    t.ma[2 + (r >> 1)] = opq((r & 1 ? t.ma[2 + (r >> 1)] : 0u) | (cb ? 0xffffu << (r & 1) * 16 : 0u));
  }
}
template <bool ATT, int N> DEVI void late_pro(CS<ATT>& t, AS3 char* sm) {
  using C = Cfg<ATT>;
  if constexpr (N == (ATT ? ESA : ESM)) { sched(); pro_pieces<ATT, C::NX + C::NW, C::NX + PRE * C::NW + 3>(t); sched(); }
  if constexpr (N == (ATT ? EPA : EPM)) { sched(); pro_pieces<ATT, C::NX + PRE * C::NW + 3, C::SC.first[1]>(t); sched(); }
  if constexpr (N < LPRO) { late<ATT, N>(t, sm); late_pro<ATT, N + 1>(t, sm); }
}
template <bool ATT, int ST, int Q> DEVI void hook(CS<ATT>& t, AS3 char* sm) {
  using C = Cfg<ATT>;
  constexpr int M = Q >= 14 ? ST + 1 : ST, R = Q >= 14 ? Q - 14 : Q + 10;
  if constexpr (C::at(M, R) >= 0) piece<ATT, C::at(M, R)>(t);
  if constexpr (M >= LS0 && R % (24 / LPI) == 2 && R / (24 / LPI) < LPI && LPRO + (M - LS0) * LPI + R / (24 / LPI) < (ATT ? 18 : 14)) late<ATT, LPRO + (M - LS0) * LPI + R / (24 / LPI)>(t, sm);
  if constexpr (!ATT && M == RINT + RD + 1 && R == 21) { t.rt[0] = ldtr(sm + t.ra); t.rt[1] = ldtr(sm + t.ra + 1024); }
  if constexpr (!ATT && M == RINT + RD + 1 && R == 22) { t.rt[2] = ldtr(sm + t.ra + 8); t.rt[3] = ldtr(sm + t.ra + 1032); }
}
template <bool ATT, int ST, int I> DEVI bf16x8 rd_w(CS<ATT>& t, AS3 char* sm) {
  if constexpr (ATT) {
    constexpr int o = (ST & 3) * 16384;
    return cat(ldtr(sm + t.wa[I] + o), ldtr(sm + t.wa[I] + o + 2048));
  } else {
    constexpr int o = (ST & 1) * 24576 + (I & 1) * 12288 + (I >> 1) * 128;
    return cat(ldtr(sm + t.wa[(ST >> 1) & 1] + o), ldtr(sm + t.wa[(ST >> 1) & 1] + o + 1536));
  }
}
template <bool ATT, int ST, int I> DEVI bf16x8 rd_x(CS<ATT>& t, AS3 char* sm) {
  return ld128(sm + t.xa[ST & 1] + ((ST >> 1) % 3) * Cfg<ATT>::XSLOT + I * 2048);
}
template <bool ATT> constexpr int ord(int k) { return ATT ? (k + 5) % 6 : k; }
template <bool ATT> constexpr int jord(int k) { return ATT ? k : (k + 3) % 4; }
template <bool ATT, int ST, int I> DEVI void rdS(CS<ATT>& t, AS3 char* sm) {
  constexpr int S2 = ST < 18 ? ST : 18 + (I & 1), I2 = ST < 18 ? I : ord<ATT>(I / 2);
  if constexpr (I < 12) {
    if constexpr (ATT) t.S[(6 * ST + I) & 3] = rd_x<ATT, S2, I2>(t, sm);
    else t.S[(6 * ST + I) & 3] = rd_w<ATT, S2, I2>(t, sm);
  }
}
template <bool ATT, int ST, int I> DEVI void rdH(CS<ATT>& t, AS3 char* sm) {
  if constexpr (ST < 20) {
    if constexpr (ATT) t.H[ST & 1][I] = rd_w<ATT, ST, I>(t, sm);
    else t.H[ST & 1][I] = rd_x<ATT, ST, I>(t, sm);
  }
}
template <class V> DEVI void pin(V& v) { asm volatile("" : "+v"(v)); }
template <bool ATT, int Q> DEVI void derive(CS<ATT>& t) {
  constexpr int I = Q / 4, NR = Cfg<ATT>::NR;
  const int lane = threadIdx.x & 63;
  if constexpr (I < NR) {
    float* r = t.rc[I];
    if constexpr (Q % 4 == 0) {
      r[0] = __uint_as_float((t.rec[I][0] << 16) ^ 0x80000000u);
      if constexpr (I + 1 == NR) r[0] = (lane & 15) == 15 ? 0.f : r[0];
      pin(r[0]);
    }
    if constexpr (Q % 4 == 1) { r[3] = hi16(t.rec[I][0]); r[1] = __builtin_amdgcn_rcpf(r[3]); pin(r[1]); pin(r[3]); }
    if constexpr (Q % 4 == 2) {
      if constexpr (ATT) { r[2] = lane < 16 ? 1.f : r[1]; r[3] = lane >= 48 ? 0.f : r[1]; }
      else { r[2] = r[1] * r[1]; r[3] *= r[3]; }
      pin(r[2]); pin(r[3]);
    }
    if constexpr (Q % 4 == 3) {
      if constexpr (ATT) {
        if constexpr (I & 1) { t.qc[I / 2] = (lane < 32 ? t.rp[I - 1][0][1] : r[1]) * (0.22360679775f * 1.44269504089f); pin(t.qc[I / 2]); }
      } else {
        r[1] *= -2.30220820f; r[2] *= r[1] * 0.044715f;
      }
      t.rp[I][0] = f32x2{r[0], r[1]}; t.rp[I][1] = f32x2{r[2], r[3]};
      pin(t.rp[I][0]); pin(t.rp[I][1]);
    }
  }
}
template <bool ATT, int N> DEVI void center(CS<ATT>& t) {
  constexpr int I = ord<ATT>(N / 8), J = jord<ATT>(N % 8 / 2), R = (N & 1) * 2;
  constexpr bool SELF = ATT ? I == 5 : J == 3;
#pragma unroll
  for (int r = R; r < R + 2; ++r) {
    float v = t.acc[I][J][r];
    if constexpr (SELF) asm volatile("v_fmac_f32_dpp %0, %0, %1 row_newbcast:15 row_mask:0xf bank_mask:0xf" : "+v"(v) : "v"(t.rp[ATT ? I : J][0][0]));
    else asm volatile("v_fmac_f32_dpp %0, %1, %2 row_newbcast:15 row_mask:0xf bank_mask:0xf" : "+v"(v) : "v"(ATT ? t.acc[5][J][r] : t.acc[I][3][r]), "v"(t.rp[ATT ? I : J][0][0]));
    t.acc[I][J][r] = v;
  }
}
#define AU0 8
template <bool ATT, int U> DEVI void epi(CS<ATT>& t, AS3 char* sm) {
  if constexpr (U >= AU0) center<ATT, U - AU0>(t);
}
template <bool ATT, int N> DEVI void center_rest(CS<ATT>& t) {
  if constexpr (N < 48) { center<ATT, N>(t); center_rest<ATT, N + 1>(t); }
}
template <bool ATT, int ST, int I, int J> DEVI void mm(CS<ATT>& t, AS3 char* sm) {
  sched();
  if constexpr (ATT) t.acc[I][J] = mfma(t.H[ST & 1][J], t.S[(6 * ST + I) & 3], t.acc[I][J]);
  else t.acc[I][J] = mfma(t.S[(6 * ST + I) & 3], t.H[ST & 1][J], t.acc[I][J]);
  sched();
  hook<ATT, ST, I * 4 + J>(t, sm);
  if constexpr (ST == 17) derive<ATT, I * 4 + J>(t);
  sched();
}
template <bool ATT, int U> DEVI void mmt(CS<ATT>& t, AS3 char* sm) {
  constexpr int G = U / 4, J = G > 0 ? jord<ATT>(U % 4) : U % 4, I = ord<ATT>(G / 2);
  sched();
  if constexpr (ATT) t.acc[I][J] = mfma(t.H[G & 1][J], t.S[G & 3], t.acc[I][J]);
  else t.acc[I][J] = mfma(t.S[G & 3], t.H[G & 1][J], t.acc[I][J]);
  sched();
  hook<ATT, 18 + U / 24, U % 24>(t, sm);
  epi<ATT, U>(t, sm);
  sched();
}
template <bool A, int G> DEVI void tail(CS<A>& t, AS3 char* sm) {
  mmt<A, 4 * G>(t, sm); rdS<A, 18, G + 3>(t, sm); mmt<A, 4 * G + 1>(t, sm);
  if constexpr (G == 9) wait_lgkm<0>();
  if constexpr (G == 3 || G == 9) {
    bar(); sched();
  }
  mmt<A, 4 * G + 2>(t, sm); mmt<A, 4 * G + 3>(t, sm);
  if constexpr (G < 11) tail<A, G + 1>(t, sm);
}
template <bool A, int ST> DEVI void stage(CS<A>& t, AS3 char* sm) {
  mm<A, ST, 0, 0>(t, sm); rdS<A, ST, 3>(t, sm); mm<A, ST, 0, 1>(t, sm); mm<A, ST, 0, 2>(t, sm); mm<A, ST, 0, 3>(t, sm);
  mm<A, ST, 1, 0>(t, sm); rdS<A, ST, 4>(t, sm); mm<A, ST, 1, 1>(t, sm); mm<A, ST, 1, 2>(t, sm); mm<A, ST, 1, 3>(t, sm);
  mm<A, ST, 2, 0>(t, sm); rdS<A, ST, 5>(t, sm); mm<A, ST, 2, 1>(t, sm); mm<A, ST, 2, 2>(t, sm); mm<A, ST, 2, 3>(t, sm);
  mm<A, ST, 3, 0>(t, sm); mm<A, ST, 3, 1>(t, sm);
  wait_lgkm<0>();
  if constexpr (ST < 19) wait_vm<Cfg<A>::vm(ST)>();
  if constexpr (ST == (A ? FCA : FCM)) flag_spin(t.wf, t.tag, t.srs, t.wfo);
  bar(); sched();
  constexpr int HL = ST == 17 ? 19 : 20;
  rdS<A, ST + 1, 0>(t, sm); mm<A, ST, 3, 2>(t, sm); rdH<A, ST + 1, 0>(t, sm); mm<A, ST, 3, 3>(t, sm); rdH<A, ST + 1, 1>(t, sm);
  mm<A, ST, 4, 0>(t, sm); rdS<A, ST + 1, 1>(t, sm); mm<A, ST, 4, 1>(t, sm); rdH<A, ST + 1, 2>(t, sm);
  mm<A, ST, 4, 2>(t, sm); rdH<A, ST + 1, 3>(t, sm); mm<A, ST, 4, 3>(t, sm);
  mm<A, ST, 5, 0>(t, sm); rdS<A, ST + 1, 2>(t, sm); rdH<A, HL, 0>(t, sm); mm<A, ST, 5, 1>(t, sm); rdH<A, HL, 1>(t, sm);
  mm<A, ST, 5, 2>(t, sm); rdH<A, HL, 2>(t, sm); mm<A, ST, 5, 3>(t, sm); rdH<A, HL, 3>(t, sm);
  if constexpr (ST == 16) { sched(); rec_test<A, false>(t); rec_retry<A>(t); sched(); }
  if constexpr (ST < 17) stage<A, ST + 1>(t, sm);
  else tail<A, 0>(t, sm);
}

struct Ptrs { const char *x, *w0, *w1, *wo, *wd; char *a2f, *w2f, *st, *out; };
typedef __attribute__((ext_vector_type(16))) u32 u32x16;
DEVI char* ka_ptr(u32x16 ka, int i) { return (char*)(((uint64_t)ka[2 * i + 1] << 32) | ka[2 * i]); }
DEVI void ka_wait(u32x16& ka, Ptrs& P, bool att) {
  asm volatile("" : "+s"(ka) :: "memory");
  char* ws = ka_ptr(ka, 6);
  P = Ptrs{ka_ptr(ka, 0), ka_ptr(ka, att ? 1 : 3), ka_ptr(ka, 4), ka_ptr(ka, 2), ka_ptr(ka, 5), ws, ws + A2F_BYTES, ws + A2F_BYTES + W2F_BYTES, ka_ptr(ka, 7)};
}

template <bool ATT> DEVI void k1_main(CS<ATT>& t, int w, int mt, int ns, int pm, u32x16& ka, Ptrs& P, AS3 char* sm) {
  using C = Cfg<ATT>;
  const int lane = threadIdx.x & 63, wh = w >> 2, wq = w & 3;
  __builtin_amdgcn_s_waitcnt(0x0F71);
  t.stok = stat_tok(blockIdx.x, w);
#pragma unroll
  for (int i = 0; i < C::NX; ++i) {
    const int r = (wq * C::NX + i) * 8 + (lane >> 3);
    int tok;
    if (ATT) tok = 2 * min(max(mt * UT_ATT - 7 + r, 0), 1023) + wh;
    else tok = tok_of(min((2 * mt + wh) * HT_MLP + r, S_ - 1));
    t.xv[i] = opq(tok * 1280 + (((lane & 7) ^ ((r >> 1) & 7)) << 4));
  }
  t.xdst = (u32)(size_t)sm + C::XR + w * (C::NX * 1024);
  t.xmask = wq == 3 ? 0x00ffffffffffffffull : ~0ull;
  if (ATT) {
#pragma unroll
    for (int i = 0; i < C::NW; ++i) {
      int row = 4 * w + 2 * i + (lane >> 5);
      int rho = (row & 3) + 4 * ((row >> 3) & 1);
      int c = ((lane & 31) - 2 * rho) & 31;
      int so = c * 16 + (c < 10 ? 2560 : c == 10 ? 2544 : c <= 20 ? -176 : c < 31 ? 944 : 928);
      t.wv[i] = opq(row * 3840 + so);
    }
  } else {
#pragma unroll
    for (int i = 0; i < C::NW; ++i) {
      int Q = i * 64 + lane;
      int r = Q / 24, c = Q - r * 24, row = wq * 8 + r;
      int u = (c >> 1) ^ ((row >> 1) & 1) ^ ((row >> 2) & 2);
      t.wv[i] = opq(row * 3456 + u * 32 + (c & 1) * 16);
    }
  }
  t.wdst = (u32)(size_t)sm + w * (C::NW * 1024);
  sched();
  ka_wait(ka, P, ATT);
  t.xrs = mkrs(P.x, S_ * D_ * 2);
  t.srs = mkrs(P.st, ST_BYTES);
  if (ATT) t.wrs = mkrs(P.w0 + ns * 160, D_ * 3 * D_ * 2 - ns * 160);
  else t.wrs = mkrs((wh ? P.w1 : P.w0) + ns * 384, D_ * F_ * 2 - ns * 384);
  pro_pieces<ATT, 0, C::NX + C::NW>(t);
  sched();
  {
    const int xcd = blockIdx.x & 7, idx = blockIdx.x >> 3, xn = xcd & 3;
    const int ju = ATT ? lane / 6 : lane >> 3;
    const int id = ATT ? (idx * 8 + w) * 6 + lane - ju * 6 : ((idx - 12) * 4 + wq) * 8 + (lane & 7);
    const int r = id / 9, l = id - r * 9;
    const int col = ATT ? ((xn * 320) & ~127) + (l / 3) * (1280 - 384) + l * 128 : 2 * xn * 384 + l * 128;
    t.prs = ATT ? mkrs(P.w0, D_ * 3 * D_ * 2) : mkrs(wh ? P.w1 : P.w0, D_ * F_ * 2);
    t.pw = ((ju * 128 + 2 * r + (xcd >> 2)) * (C::WSTEP / 32) + col) | (u32)(63 - r) & OOB;
  }
  t.l16 = lane * 16;
  t.w = w; t.mt = mt; t.pm = pm; t.wo = P.wo; t.wd = P.wd; t.w2f = P.w2f;
  sched();
  if (wq == 3 && lane < 24) {
    u32x4 ones = {0x3f803f80u, 0x3f803f80u, 0x3f803f80u, 0x3f803f80u};
    *(AS3 u32x4*)(sm + C::XR + (lane >> 3) * C::XSLOT + (w + 1) * (C::NX * 1024) - 128 + (lane & 7) * 16) = ones;
  }
#pragma unroll
  for (int i = 0; i < 6; ++i)
#pragma unroll
    for (int c = 0; c < 4; ++c) t.acc[i][c] = f32x4{0.f, 0.f, 0.f, 0.f};
  asm volatile("" : "+v"(t.pw), "+v"(t.l16));
  late_pro<ATT, 0>(t, sm);
  sched();
  wait_lgkm<0>();
  wait_vm<C::vm(-1)>();
  bar();
  sched();
  rdS<ATT, 0, 0>(t, sm); rdH<ATT, 0, 0>(t, sm); rdH<ATT, 0, 1>(t, sm); rdH<ATT, 0, 2>(t, sm); rdH<ATT, 0, 3>(t, sm);
  rdS<ATT, 0, 1>(t, sm); rdS<ATT, 0, 2>(t, sm);
  stage<ATT, 0>(t, sm);
  wp_ld(t.wp, 0, E0, t.vrs, t.l16, wo_of(w));
  sched();
}

#define EML 11
#define EAL 16
constexpr int em(int k) { return k >= EML ? NE : E0 + (NE - E0) * k / EML; }
constexpr int ea(int k) { return k >= EAL ? NE : E0 + (NE - E0) * k / EAL; }
DEVI u32 k1_mlp(WPre& wp, u32 dummy, int w, int mt, int fs, int pm, u32x16& ka, Ptrs& P, AS3 char* sm) {
  using C = Cfg<false>;
  const int lane = threadIdx.x & 63, wm = w >> 2, wn = w & 3;
  const int G = lane >> 4, tq = lane & 15;
  CS<false> t;
  {
    int q = (lane >> 2) & 3, p = lane & 3;
    t.wa[0] = opq((8 * G + q) * 384 + ((wn ^ (q >> 1) ^ ((G & 1) << 1)) << 5) + 8 * p);
    t.wa[1] = opq(t.wa[0] + 49152);
    t.xa[0] = opq(C::XR + (wm * 64 + tq) * 128 + ((G ^ (tq >> 1)) << 4));
    t.xa[1] = opq(t.xa[0] ^ 64);
  }
  t.dummy = dummy;
  k1_main<false>(t, w, mt, fs, pm, ka, P, sm);
  char* a2f = P.a2f;
  const rsrc_t ors = mkrso(a2f, A2F_BYTES, (20 + fs * 6 + wn) * 4096);
  const rsrc_t ors2 = mkrso(a2f, A2F_BYTES, (24 + fs * 6 + (wn >> 1)) * 4096 + (wn & 1) * 512);
  center_rest<false, 48 - AU0>(t);
  u32 hp[3][4][2];
#pragma unroll
  for (int b = 0; b < 3; ++b) {
#pragma unroll
    for (int i = 0; i < 4; ++i) {
      const f32x2 ca = t.rp[i][0].yy, cb = t.rp[i][1].xx, cd = t.rp[i][1].yy;
#pragma unroll
      for (int e = 0; e < 2; ++e) {
        f32x2 g = e ? half2<1>(t.acc[2 * b][i]) : half2<0>(t.acc[2 * b][i]);
        f32x2 u = e ? half2<1>(t.acc[2 * b + 1][i]) : half2<0>(t.acc[2 * b + 1][i]);
        f32x2 z = g * fma2(g * g, cb, ca);
        f32x2 ex = {__builtin_amdgcn_exp2f(z[0]), __builtin_amdgcn_exp2f(z[1])};
        f32x2 d = fma2(ex, cd, cd);
        f32x2 rd = {__builtin_amdgcn_rcpf(d[0]), __builtin_amdgcn_rcpf(d[1])};
        hp[b][i][e] = pk2v(g * u * rd);
      }
      if (b == 1) st128(u32x4{hp[0][i][0], hp[0][i][1], hp[1][i][0], hp[1][i][1]}, ors, t.vo[i]);
      if (b == 2 && (i & 1))
        st128(pair_rows(hp[2][i - 1][0], hp[2][i - 1][1], hp[2][i][0], hp[2][i][1]), ors2, t.vo[4 + i / 2]);
    }
  }
  wp = t.wp;
  return t.tag;
}

#define VDT 4
#define LATEV 2
DEVI void v_six(rsrc_t vrs, u32 l16, u32 vdst) {
  dma16c<VC0 * 8192>(vrs, l16, v_soff(VC0), vdst); dma16c<(VC0 + 1) * 8192>(vrs, l16, v_soff(VC0 + 1), vdst);
  dma16c<(VC0 + 2) * 8192>(vrs, l16, v_soff(VC0 + 2), vdst); dma16c<(VC0 + 3) * 8192>(vrs, l16, v_soff(VC0 + 3), vdst);
  dma16c<(VC0 + 4) * 8192>(vrs, l16, v_soff(VC0 + 4), vdst); dma16c<(VC0 + 5) * 8192>(vrs, l16, v_soff(VC0 + 5), vdst);
}
DEVI float fmx(float a, float b) { return __builtin_elementwise_maximum(a, b); }
DEVI u32 k1_att(WPre& wp, u32 dummy, int w, int mt, int hg, u32x16& ka, Ptrs& P, AS3 char* sm) {
  using C = Cfg<true>;
  const int lane = threadIdx.x & 63, hl = w & 3, par = w >> 2;
  const int G = lane >> 4, tq = lane & 15, q = (lane >> 2) & 3, p = lane & 3;
  CS<true> t;
  {
    int rho = q + 4 * (G & 1);
    int rowb = (8 * G + q) * 512;
    int hb = hl * 40;
    int o0 = 176 + 8 * p, o1 = 336 + 8 * p;
    int o2 = p == 0 ? 176 + 32 : p == 1 ? 0 : p == 2 ? 8 : 24;
    int o3 = p == 0 ? 336 + 32 : p == 1 ? 32 : p == 2 ? 16 : 8;
    t.wa[0] = opq(rowb + ((o0 + hb + 32 * rho) & 511));
    t.wa[1] = opq(rowb + ((o1 + hb + 32 * rho) & 511));
    t.wa[2] = opq(rowb + ((o2 + hb + 32 * rho) & 511));
    t.wa[3] = opq(rowb + ((o3 + hb + 32 * rho) & 511));
    t.xa[0] = opq(C::XR + (par * 96 + tq) * 128 + ((G ^ (tq >> 1)) << 4));
    t.xa[1] = opq(t.xa[0] ^ 64);
  }
  t.dummy = dummy;
  k1_main<true>(t, w, mt, hg, 0, ka, P, sm);
  char* a2f = P.a2f;
  center_rest<true, 48 - AU0>(t);
  u32x4 qf[6], kf[6];
#pragma unroll
  for (int T = 0; T < 6; ++T) {
    const f32x2 rs = t.rp[T][0].yy, r2 = t.rp[T][1].xx, r3 = t.rp[T][1].yy;
    const u32x2 c2 = {pk2v(half2<0>(t.acc[T][2]) * r2), pk2v(half2<1>(t.acc[T][2]) * r2)};
    const u32x2 c3 = {pk2v(half2<0>(t.acc[T][3]) * r3), pk2v(half2<1>(t.acc[T][3]) * r3)};
    *(AS3 u32x2*)(sm + t.vsa + T * 8192) = c2;
    *(AS3 u32x2*)(sm + t.vsa + T * 8192 + 512) = c3;
    qf[T] = u32x4{pk2v(half2<0>(t.acc[T][0])), pk2v(half2<1>(t.acc[T][0])), c2[0] & t.g0m, c2[1] & t.g0m};
    kf[T] = u32x4{pk2v(half2<0>(t.acc[T][1]) * rs), pk2v(half2<1>(t.acc[T][1]) * rs), c3[0], c3[1]};
  }
  bf16x8 v0[6], v1[6];
#pragma unroll
  for (int T = 0; T < 6; ++T) {
    const int o = T * 8192, po = T ? o - 8192 : 0;
    v0[T] = cat(ldtr(sm + t.va[4] + o), ldtr(sm + opq(t.va[4]) + po));
    v1[T] = cat(ldtr(sm + t.va[T & 3] + o), ldtr(sm + opq(t.va[T & 3]) + po));
  }
  const f32x4 z4 = {0.f, 0.f, 0.f, 0.f};
  const int H = hg * 4 + hl;
  const rsrc_t ors = mkrso(a2f, A2F_BYTES, (H >> 1) * 4096 + (H & 1) * 512);
  const rsrc_t ors2 = mkrso(a2f, A2F_BYTES, (16 + (H >> 3)) * 4096 + ((H >> 1) & 3) * 256 + (H & 1) * 8);
  u32x4 pf[6];
#pragma unroll
  for (int T = 0; T < 6; T += 2) {
    f32x4 s0 = mfmau(kf[T], qf[T], T ? t.ba : t.b0), s1 = mfmau(kf[T + 1], qf[T + 1], t.ba), s;
    const f32x4 s3 = mfmau(kf[T], qf[T + 1], t.bb);
    if (T) s0 = __builtin_elementwise_maximum(s0, mfmau(kf[T ? T - 1 : 0], qf[T], t.bb));
    s1 = __builtin_elementwise_maximum(s1, s3);
#pragma unroll
    for (int r = 0; r < 4; ++r) {
      auto x = __builtin_amdgcn_permlane32_swap(__float_as_uint(s0[r]), __float_as_uint(s1[r]), false, false);
      s[r] = fmx(__uint_as_float(x[0]), __uint_as_float(x[1]));
    }
    float mx = fmx(fmx(s[0], s[1]), fmx(s[2], s[3]));
    auto x = __builtin_amdgcn_permlane16_swap(__float_as_uint(mx), __float_as_uint(mx), false, false);
    mx = fmx(__uint_as_float(x[0]), __uint_as_float(x[1]));
    const float c = t.qc[T / 2];
    f32x2 e0 = fma2(half2<0>(s), bc2(c), bc2(-mx * c)), e1 = fma2(half2<1>(s), bc2(c), bc2(-mx * c));
    e0 = f32x2{__builtin_amdgcn_exp2f(e0[0]), __builtin_amdgcn_exp2f(e0[1])};
    e1 = f32x2{__builtin_amdgcn_exp2f(e1[0]), __builtin_amdgcn_exp2f(e1[1])};
    const f32x2 a = e0 + e1;
    const float su = a[0] + a[1];
    auto y = __builtin_amdgcn_permlane16_swap(__float_as_uint(su), __float_as_uint(su), false, false);
    const f32x2 inv = bc2(__builtin_amdgcn_rcpf(__uint_as_float(y[0]) + __uint_as_float(y[1])));
    const u32 p0 = pk2v(e0 * inv), p1 = pk2v(e1 * inv);
    auto q0 = __builtin_amdgcn_permlane32_swap(p0, p0, false, false);
    auto q1 = __builtin_amdgcn_permlane32_swap(p1, p1, false, false);
    pf[T] = u32x4{q0[0] & t.ma[0], q1[0] & t.ma[1], q0[0] & t.ma[2], q1[0] & t.ma[3]};
    pf[T + 1] = u32x4{q0[1] & t.ma[0], q1[1] & t.ma[1], q0[1] & t.ma[2], q1[1] & t.ma[3]};
  }
  f32x4 o1;
#pragma unroll
  for (int T = 0; T < 6; ++T) {
    const bf16x8 pb = __builtin_bit_cast(bf16x8, pf[T]);
    const f32x4 o0 = mfma(v0[T], pb, z4);
    o1 = mfma(v1[T], pb, (T & 3) ? o1 : z4);
    __builtin_amdgcn_raw_buffer_store_b64(u32x2{pk2v(half2<0>(o0)), pk2v(half2<1>(o0))}, ors, t.vo[T == 5 ? 3 : T < 3 ? T : T - 2],
                                          T == 3 || T == 4 ? KB2 * 4096 : 0, ST_AUX);
    if (T == 3 || T == 5) st64(u32x2{pk2v(half2<0>(o1)), pk2v(half2<1>(o1))}, ors2, t.vo[T == 3 ? 4 : 5]);
  }
  wp = t.wp;
  return t.tag;
}

#define A_AUX 16
#define RB 240
#define AWMAX 6
constexpr int NJ = 37;
#define J0 8
#define J1 17
#define WQ0 11
#define EB 31
constexpr int job_s(int q) { return q == 0 ? 9 : q < 21 ? (q - 1) / 4 : 5 + (q - 21) % 4; }
constexpr int job_i(int q) { return q == 0 ? 0 : q < 21 ? (q - 1) % 4 : (q - 21) / 4; }
struct P2 {
  f32x4 acc[4][5], r2, pr[10];
  u32x4 a[NJ], wv[10][5], xr0;
  rsrc_t ar, srs;
  u32 l16, so[4], xo, lw[3], ra[4][3], tag, po2, pv;
};
template <int Q> DEVI void p2_lda(P2& t) {
  if constexpr (Q < NJ) {
    constexpr int S = job_s(Q), I = job_i(Q);
    t.a[Q] = __builtin_amdgcn_raw_buffer_load_b128(t.ar, t.l16, S == 9 ? t.xo : t.so[I] + grp_of(grp_tab(), S) * 32768, A_AUX);
  }
}
constexpr int a_lim(int q) {
  const int r = q < J1 ? 22 : NJ;
  return r < AWMAX + q ? r : AWMAX + q;
}
template <int F, int L> DEVI void p2_ldr(P2& t) {
  if constexpr (F < L) { p2_lda<F>(t); p2_ldr<F + 1, L>(t); }
}
template <int C> DEVI AS3 char* p2_own(P2& t, AS3 char* sm) { return sm + t.lw[C / 8] + (C % 8) * 8192; }
template <int P> DEVI void p2_rdw(P2& t, AS3 char* sm) {
  if constexpr (P >= 0 && P < 20) t.wv[5 + P / 5][P % 5] = *(AS3 u32x4*)p2_own<P>(t, sm);
}
template <int I, int N> DEVI void p2_wr(P2& t, AS3 char* sm) { *(AS3 f32x4*)p2_own<I * 5 + N>(t, sm) = t.acc[I][N]; }
template <int T, int K> DEVI f32x4 p2_part(P2& t, AS3 char* sm) { return *(AS3 f32x4*)(sm + t.ra[T][K / 2] + K * 8192); }
DEVI void add4(f32x4& a, f32x4 b) {
#pragma unroll
  for (int r = 0; r < 4; ++r) asm("v_add_f32 %0, %0, %1" : "+v"(a[r]) : "v"(b[r]));
}
template <int K> DEVI void p2_add(P2& t) {
  if constexpr (K >= 0 && K < 10) add4(K % 5 == 4 ? t.r2 : t.acc[3][K % 5 & 1], t.pr[K]);
}
template <int M> DEVI void p2_hook(P2& t, AS3 char* sm) {
  constexpr int Q = M / 5, N = M % 5;
  if constexpr (N == 1) p2_rdw<(Q - WQ0) * 2>(t, sm);
  if constexpr (N == 3) p2_rdw<(Q - WQ0) * 2 + 1>(t, sm);
  if constexpr (M >= 125 && M < 165 && (M - 125) % 20 % 3 == 1 && (M - 125) % 20 < 15) p2_wr<(M - 125) / 20, (M - 125) % 20 / 3>(t, sm);
  if constexpr (M >= 165 && M < 175 && (M & 1) == 0) p2_wr<2, (M - 165) / 2>(t, sm);
  if constexpr (M == 178 || M == 180 || M == 182) p2_wr<3, 2 + (M - 178) / 2>(t, sm);
  constexpr int E = M - (EB + 1) * 5;
  if constexpr (E >= 0 && E < 5) { t.pr[2 * E] = p2_part<2 * E / 5, 2 * E % 5>(t, sm); t.pr[2 * E + 1] = p2_part<(2 * E + 1) / 5, (2 * E + 1) % 5>(t, sm); }
  p2_add<M - 167>(t);
}
template <int M> DEVI void p2_mm(P2& t, AS3 char* sm) {
  constexpr int L = M - 165, Q = L < 0 ? M / 5 : L < 12 ? 33 + L / 3 : 33 + (L - 12) / 2, N = L < 0 ? M % 5 : L < 12 ? 2 + L % 3 : (L - 12) % 2;
  constexpr int S = job_s(Q), I = job_i(Q);
  f32x4 c = t.acc[I][N];
  if constexpr (Q == 0 || (Q > 1 && Q <= 4)) c = f32x4{0.f, 0.f, 0.f, 0.f};
  if constexpr (Q == 4 && N < 2) c = f32x4{lo16(t.xr0[2 * N]), hi16(t.xr0[2 * N]), lo16(t.xr0[2 * N + 1]), hi16(t.xr0[2 * N + 1])};
  t.acc[I][N] = mfmau(t.wv[S][N], t.a[Q], c);
  sched();
  p2_hook<M>(t, sm);
  sched();
}
template <int Q> DEVI void p2_job(P2& t, AS3 char* sm) {
  p2_mm<Q * 5>(t, sm); p2_mm<Q * 5 + 1>(t, sm); p2_mm<Q * 5 + 2>(t, sm); p2_mm<Q * 5 + 3>(t, sm); p2_mm<Q * 5 + 4>(t, sm);
  if constexpr (Q + 1 == J1) { flag_spin(t.pv, t.tag, t.srs, t.po2); sched(); }
  p2_ldr<a_lim(Q), a_lim(Q + 1)>(t);
  if constexpr (Q == J0) { sched(); t.pv = __builtin_amdgcn_raw_buffer_load_b32(t.srs, t.po2, 0, 16); }
  if constexpr (Q == EB) { sched(); wait_lgkm<0>(); bar(); }
  sched();
  if constexpr (Q + 1 < NJ) p2_job<Q + 1>(t, sm);
}

DEVI void phase2(WPre& wp, int w, u32 tag, u32 fo, const Ptrs& P, char* out, AS3 char* sm) {
  const int lane = threadIdx.x & 63, mi = mi_of(blockIdx.x), ni = ni_of(blockIdx.x);
  P2 t;
  sched();
  t.l16 = lane * 16;
  const rsrc_t wr = mkrso(P.w2f, W2F_BYTES, (ni * KB2 + w) * 5120);
  const int d = w >> 1, h = w & 1, G = lane >> 4, fl = h * 32 + (lane & 31);
  const rsrc_t xrs = mkrso(P.x, S_ * D_ * 2, ni * 160), ors = mkrso(out, S_ * D_ * 2, ni * 160);
  const u32 ro = tok_of(mi * 64 + d * 16 + (lane & 15)) * 1280;
  const u32 o1 = ro + h * 64 + G * 16, o2 = lane < 32 ? ro + 128 + (fl >> 4) * 8 : OOB;
  t.xr0 = __builtin_amdgcn_raw_buffer_load_b128(xrs, o1, 0, 0);
  const u32x2 xr2 = __builtin_amdgcn_raw_buffer_load_b64(xrs, o2, 0, 0);
  const rsrc_t srs = mkrs(P.st, ST_BYTES);
  u32 po;
  {
    const int ma = (mi * 64) / 122, mb = (mi * 64 + 63) / 122, aa = (mi * 32) / 86, ab = (mi * 32 + 31) / 86;
    const int c0 = (ma & 1) * 9, c1 = (mb & 1) * 9 - 9, c2 = (aa & 1) * 8, c3 = (ab & 1) * 8 - 8;
    po = opq(AF_OFF + mi * 4096 + (min(lane, 17) + (lane < 9 ? c0 : c1)) * FS);
    t.po2 = opq(AF_OFF + mi * 4096 + (18 + min(lane, 15) + (lane < 8 ? c2 : c3)) * FS);
    fo = opq(fo);
  }
  t.ar = mkrso(P.a2f, A2F_BYTES, (mi * KB2 + w) * 4096);
#pragma unroll
  for (int i = 0; i < 4; ++i) t.so[i] = ((i + 1 + d) & 3) * 1024;
  t.xo = __builtin_amdgcn_readfirstlane((72 + h - w) * 4096 + t.so[0]);
  asm volatile("" :: "s"(t.so[0]), "s"(t.so[1]), "s"(t.so[2]), "s"(t.so[3]), "s"(t.xo), "s"(t.ar), "s"(srs));
  sched();
  wait_vm<2>();
  bar();
  u32 pv = ~tag;
  if (w == 0) {
    st32(tag, srs, fo);
    flag_load(pv, srs, po);
  }
  sched();
  wp_ld(wp, NE, 30, wr, t.l16, wo_of(w));
  sched();
  if (blockIdx.x < 96) v_six(wr, t.l16, (u32)(size_t)sm + w * 1024);
#pragma unroll
  for (int e = 0; e < 30; ++e) t.wv[e < 5 ? 9 : e / 5 - 1][e % 5] = wp.v[e];
  t.srs = srs; t.tag = tag;
  sched();
  if (w == 0) {
    asm volatile("s_waitcnt vmcnt(%1)" : "+v"(pv) : "n"(30 - NE) : "memory");
    flag_spin(pv, tag, srs, po);
  }
  bar();
  sched();
  p2_ldr<0, a_lim(0)>(t);
  sched();
#pragma unroll
  for (int i = 0; i < 3; ++i) t.lw[i] = opq(i * 65536 + w * 1024 + lane * 16);
#pragma unroll
  for (int i = 0; i < 4; ++i) {
    const int b = i * 40960 + ((d - i - 1) & 3) * 2048;
    if (i < 3) t.ra[i][0] = opq(b + h * 1024 + lane * 16);
    t.ra[i][1] = opq(b + (h ^ 1) * 1024 + lane * 16);
    t.ra[i][2] = opq(b + (lane >> 5) * 1024 + fl * 16);
  }
  t.r2 = f32x4{lo16(xr2[0]), hi16(xr2[0]), lo16(xr2[1]), hi16(xr2[1])};
  sched();
  p2_job<0>(t, sm);
  wait_lgkm<0>();
  bar();
  sched();
  const f32x4 q4 = p2_part<2, 4>(t, sm), s4 = p2_part<3, 4>(t, sm);
  const f32x4 q0 = p2_part<2, 0>(t, sm), q2 = p2_part<2, 2>(t, sm), s2 = p2_part<3, 2>(t, sm);
  const f32x4 q1 = p2_part<2, 1>(t, sm), q3 = p2_part<2, 3>(t, sm), s3 = p2_part<3, 3>(t, sm);
  sched();
  f32x4 r2 = t.r2 + q4 + s4;
#pragma unroll
  for (int r = 0; r < 4; ++r) {
    auto s = __builtin_amdgcn_permlane32_swap(__float_as_uint(r2[r]), __float_as_uint(r2[r]), false, false);
    r2[r] = __uint_as_float(s[0]) + __uint_as_float(s[1]);
  }
  __builtin_amdgcn_raw_buffer_store_b64(u32x2{pk2(r2[0], r2[1]), pk2(r2[2], r2[3])}, ors, o2, 0, 0);
  const f32x4 r0 = t.acc[3][0] + q0 + q2 + s2, r1 = t.acc[3][1] + q1 + q3 + s3;
  __builtin_amdgcn_raw_buffer_store_b128(u32x4{pk2(r0[0], r0[1]), pk2(r0[2], r0[3]), pk2(r1[0], r1[1]), pk2(r1[2], r1[3])}, ors, o1, 0, 0);
}
template <int P> DEVI void v_all(rsrc_t vrs, u32 l16, u32 dst) {
  dma16c<P * 8192>(vrs, l16, v_soff(P), dst);
  if constexpr (P < 19) v_all<P + 1>(vrs, l16, dst);
}

#define EXPECT0(c) __builtin_expect(c, 0)
__global__ __launch_bounds__(512) void k1(const char*, const char*, const char*, const char*, const char*, const char*, char*, char*) {
  __shared__ __attribute__((aligned(1024))) char smem[LDS_K1];
  AS3 char* sm = (AS3 char*)smem;
  u32x16 ka = *(__attribute__((address_space(4))) const volatile u32x16*)__builtin_amdgcn_kernarg_segment_ptr();
  sched();
  const int b = blockIdx.x, xcd = b & 7, idx = b >> 3;
  const int xm = xcd >> 2, xn = xcd & 3;
  const int w = __builtin_amdgcn_readfirstlane(threadIdx.x >> 6), lane = threadIdx.x & 63;
  u32 dummy = 0;
  if (CODEB) {
    uint64_t pc;
    asm volatile("s_getpc_b64 %0" : "=s"(pc));
    touch<2, NTS(6)>(dummy, mkrs((const char*)(pc & ~255ull), CODEB), lane * 128, (idx * 8 + w) * 256);
  }
  Ptrs P;
  u32 tag;
  WPre wp;
  int g0, g1, slot;
  const int j = idx - 12;
  if (idx < 12) {
    const int mt = xm * 6 + (idx >> 1), hg = xn * 2 + (idx & 1);
    tag = k1_att(wp, dummy, w, mt, hg, ka, P, sm);
    g0 = (mt * UT_ATT) >> 5; g1 = min(mt * UT_ATT + UT_ATT - 1, 1023) >> 5; slot = 18 + (mt & 1) * 8 + hg;
  } else
  if (EXPECT0((xm == 1) & (j >= (xn == 3 ? 19 : 18)))) {
    ka_wait(ka, P, false);
    char* w2f = P.w2f;
    const rsrc_t srs = mkrs(P.st, ST_BYTES);
    StatLd sl;
    stat_load(sl, mkrs(P.x, S_ * D_ * 2), srs, stat_tok(b, w), lane);
    const int sg = xn * 2 + j - (xn == 3 ? 19 : 18), kb = KBM + sg * 4 + (w >> 1);
    const RtK k = rt_rows(kb);
    const rsrc_t rrs = mkrso(P.wd, F_ * D_ * 2, k.base * 1280 + (w & 1) * 640);
#pragma unroll 1
    for (int i = 0; i < 20; ++i) {
      const int c = i * 64 + lane, r = c / 40;
      dma16<0, false>(rrs, ((r & 3) + k.c2 * ((r >> 2) & 1) + k.c3 * ((r >> 3) & 1) + k.c4 * (r >> 4)) * 1280 + (c - r * 40) * 16, 0,
                      (u32)(size_t)sm + w * 20480 + i * 1024);
    }
    tag = stat_pub<(SPO < 2 ? SPN : 0) + SPW + 20>(sl, srs, stat_tok(b, w), lane);
    wait_vm<1 + (SPO == 1 ? SPN : 0)>();
#pragma unroll 1
    for (int i = 0; i < 4; ++i) {
      const rsrc_t ors = mkrso(w2f, W2F_BYTES, (((w & 1) * 4 + i) * KB2 + kb) * 5120);
      AS3 char* ra = sm + w * 20480 + i * 160 + (8 * (lane >> 4) + ((lane >> 2) & 3)) * 640 + (lane & 3) * 16;
      const u32 o = lane * 16 + (kb & 1) * 2048;
      st128(__builtin_bit_cast(u32x4, cat(ldtr(ra), ldtr(ra + 2560))), ors, o);
      st128(__builtin_bit_cast(u32x4, cat(ldtr(ra + 8), ldtr(ra + 2568))), ors, o + 1024);
      st128(__builtin_bit_cast(u32x4, cat(ldtr(ra + 64), ldtr(ra + 2624))), ors, o ^ 2048);
      st128(__builtin_bit_cast(u32x4, cat(ldtr(ra + 72), ldtr(ra + 2632))), ors, (o ^ 2048) + 1024);
      ra += 128 - (lane & 3) * 8;
      st128(__builtin_bit_cast(u32x4, cat(ldtr(ra), ldtr(ra + 2560))), ors, lane * 16 + 4096);
    }
    wait_vm<SPO == 2 ? SPN : 0>();
    bar();
    st32(tag, srs, threadIdx.x == 0 ? WF_OFF + sg * 64 : OOB);
    {
      const int v = (xn * 2 + j - (xn == 3 ? 19 : 18)) * 8 + w, lr = v < 20 ? 30 : 27, pr = lr * 128 / 3;
      const rsrc_t prs = mkrs(v < 20 ? ka_ptr(ka, 1) : v < 38 ? P.w0 : P.w1, D_ * lr * 128);
      const u32 vo = (640 * lr - SPN * pr + (v < 20 ? v : v < 38 ? v - 20 : v - 38) * 64 + lane) * 128;
#pragma unroll
      for (int r = 0; r < SPN; ++r)
        asm volatile("buffer_load_dword %0, %1, %2, %3 offen" : "+v"(dummy) : "v"(vo), "s"(prs), "s"(r * pr * 128) : "memory");
    }
    if (b == 255) {
#pragma unroll
      for (int i = 0; i < 4; ++i) flag_spin(~tag, tag, srs, (i * 512 + threadIdx.x) * 8 + 4);
      bar();
      st32(tag, srs, threadIdx.x == 0 ? S_ * 8 : OOB);
    }
    flag_spin(~tag, tag, srs, wf_off(ni_of(b), lane));
    asm volatile("" :: "v"(dummy));
    v_all<0>(mkrso(w2f, W2F_BYTES, (ni_of(b) * KB2 + w) * 5120), lane * 16, (u32)(size_t)sm + w * 1024);
    wp_ld(wp, 0, NE, mkrso(w2f, W2F_BYTES, (ni_of(b) * KB2 + w) * 5120), lane * 16, wo_of(w));
    __builtin_amdgcn_s_waitcnt(0x0F70);
    g0 = 1; g1 = 0; slot = 0;
  } else {
    int mt, fs;
    if (xm == 0) {
      const int q = xn * 20 + j;
      mt = q % 9; fs = q / 9;
    } else {
      const int e = xn * 18 + j;
      mt = e < 72 ? 9 + (e & 7) : 8; fs = e < 72 ? e >> 3 : 8;
    }
    tag = k1_mlp(wp, dummy, w, mt, fs, xm ? xn * 18 + j : 73 + j * 4 + xn, ka, P, sm);
    g0 = (mt * 2 * HT_MLP) >> 6; g1 = min(mt * 2 * HT_MLP + 2 * HT_MLP - 1, S_ - 1) >> 6; slot = (mt & 1) * 9 + fs;
  }
  phase2(wp, w, tag, g0 + lane <= g1 ? AF_OFF + (g0 + lane) * 4096 + slot * FS : OOB, P, P.out, sm);
}

extern "C" void kernel_launch(void* const* d_in, const int* in_sizes, int n_in, void* d_out, int out_size,
                              void* d_ws, size_t ws_size, hipStream_t stream) {
  k1<<<256, 512, 0, stream>>>((const char*)d_in[0], (const char*)d_in[1], (const char*)d_in[2], (const char*)d_in[4],
                              (const char*)d_in[5], (const char*)d_in[6], (char*)d_ws, (char*)d_out);
}
```
